```python
import torch

def _softmax(x, axis=-1):
    return torch.softmax(x, dim=axis)
def _gelu(x):
    return torch.nn.functional.gelu(x, approximate="tanh")
def _silu(x):
    return torch.nn.functional.silu(x)
def _sigmoid(x):
    return torch.sigmoid(x)
def _layernorm(x, axis=-1, eps=1e-5):
    m = x.mean(dim=axis, keepdim=True)
    v = x.var(dim=axis, keepdim=True, unbiased=False)
    return (x - m) / torch.sqrt(v + eps)
def _scatter_add(tgt, idx, src, axis=0):
    return tgt.clone().index_add_(axis, idx, src)
def _segment_sum(src, seg, n):
    out = torch.zeros((n,) + src.shape[1:], dtype=src.dtype)
    return out.index_add_(0, seg, src)
def _pad(x, pw):
    flat = tuple(v for pair in reversed(pw) for v in pair)
    return torch.nn.functional.pad(x, flat)

kernel_name = 'interleave_layers__gqa_block_cached__mixer_channel_mlp__decode'

def setup_inputs():

    _w = (lambda a: (lambda t: t.to(torch.bfloat16) if t.is_floating_point() else t)(torch.as_tensor(a)))
    def _A_setup():
        B, Sq, D = 1, 1, 640
        Hq, Hkv, Sk, Dh = 32, 32, 4096, 128
        assert Hq % Hkv == 0, (Hq, Hkv)
        return {
            "x": _w((lambda *s: torch.randn(*s, dtype=torch.bfloat16) * 0.05)(B, Sq, D)),
            "k_cache": _w((lambda *s: torch.randn(*s, dtype=torch.bfloat16) * 3.2)(B, Hkv, Sk, Dh)),
            "v_cache": _w((lambda *s: torch.randn(*s, dtype=torch.bfloat16) * 0.4)(B, Hkv, Sk, Dh)),
            "wq": _w((lambda *s: torch.randn(*s, dtype=torch.bfloat16) * 3.2)(D, Hq * Dh) * (D ** -0.5)),
            "wo": _w((lambda *s: torch.randn(*s, dtype=torch.bfloat16) * 0.4)(Hq * Dh, D) * ((Hq * Dh) ** -0.5)),
        }
    _A_in = _A_setup()

    def _B_setup():
        B, S, D, F = 1, 1, 640, 1728
        return {
            "x": _w((lambda *s: torch.randn(*s, dtype=torch.bfloat16) * 0.05)(B, S, D)),
            "w1": _w((lambda *s: torch.randn(*s, dtype=torch.bfloat16) * 0.4)(D, F) * (D ** -0.5)),
            "b1": _w((lambda *s: torch.randn(*s, dtype=torch.bfloat16) * 0.4)(F)),
            "w2": _w((lambda *s: torch.randn(*s, dtype=torch.bfloat16) * 0.4)(F, D) * (F ** -0.5)),
            "b2": _w((lambda *s: torch.randn(*s, dtype=torch.bfloat16) * 0.2)(D)),
        }
    _B_in = _B_setup()

    return {
        "A_x": _A_in["x"],
        "A_k_cache": _A_in["k_cache"],
        "A_v_cache": _A_in["v_cache"],
        "A_wq": _A_in["wq"],
        "A_wo": _A_in["wo"],
        "B_x": _B_in["x"],
        "B_w1": _B_in["w1"],
        "B_b1": _B_in["b1"],
        "B_w2": _B_in["w2"],
        "B_b2": _B_in["b2"],
    }

def reference(A_x, A_k_cache, A_v_cache, A_wq, A_wo, B_x, B_w1, B_b1, B_w2, B_b2):
    def _A_ref(x, k_cache, v_cache, wq, wo):
        B, Sq, D = x.shape
        Hq, Dh = 32, 128
        Hkv = k_cache.shape[1]
        rep = Hq // Hkv
        q = (lambda x, *d: x.permute(*d))((x @ wq).reshape(B, Sq, Hq, Dh), 0, 2, 1, 3)
        k = (lambda x, n, axis: torch.repeat_interleave(x, n, dim=axis))(k_cache, rep, axis=1)
        v = (lambda x, n, axis: torch.repeat_interleave(x, n, dim=axis))(v_cache, rep, axis=1)
        scores = torch.einsum("bhqd,bhkd->bhqk", q, k) * (Dh ** -0.5)
        p = _softmax(scores, axis=-1)
        o = torch.einsum("bhqk,bhkd->bhqd", p, v)
        return (lambda x, *d: x.permute(*d))(o, 0, 2, 1, 3).reshape(B, Sq, Hq * Dh) @ wo

    def _B_ref(x, w1, b1, w2, b2):
        y = _layernorm(x, axis=-1)
        h = _gelu(torch.einsum("bsd,df->bsf", y, w1) + b1)
        return torch.einsum("bsf,fd->bsd", h, w2) + b2

    x = A_x
    x = x + _A_ref(_layernorm(x, axis=-1), A_k_cache, A_v_cache, A_wq, A_wo)
    x = x + _B_ref(_layernorm(x, axis=-1), B_w1, B_b1, B_w2, B_b2)
    x = x + _A_ref(_layernorm(x, axis=-1), A_k_cache, A_v_cache, A_wq, A_wo)
    x = x + _B_ref(_layernorm(x, axis=-1), B_w1, B_b1, B_w2, B_b2)
    return x
```

```cpp
#include <hip/hip_runtime.h>
#include <hip/hip_bf16.h>
#include <stdint.h>
#define ROUND 0
typedef unsigned short u16;
__device__ __forceinline__ float bf2f(u16 v) { return __uint_as_float(((unsigned)v) << 16); }
__device__ __forceinline__ float rbf(float f) {
  return f;
}
constexpr int D = 640, H = 32, SK = 4096, DH = 128, F = 1728, NS = 8;

__device__ float block_sum(float v, float* red) {
  for (int o = 32; o > 0; o >>= 1) v += __shfl_xor(v, o);
  int w = threadIdx.x >> 6, nw = blockDim.x >> 6;
  __syncthreads();
  if ((threadIdx.x & 63) == 0) red[w] = v;
  __syncthreads();
  float s = 0; for (int i = 0; i < nw; ++i) s += red[i];
  return s;
}
__device__ void ln_shared(float* xs, float* red) {
  float s = 0; for (int i = threadIdx.x; i < D; i += blockDim.x) s += xs[i];
  s = block_sum(s, red); float mean = s / D;
  float v = 0; for (int i = threadIdx.x; i < D; i += blockDim.x) { float d = xs[i] - mean; v += d * d; }
  v = block_sum(v, red) / D;
  float mb = rbf(mean), vb = rbf(v);
  float sd = rbf(sqrtf(rbf(vb + 1e-5f)));
  __syncthreads();
  for (int i = threadIdx.x; i < D; i += blockDim.x) xs[i] = rbf(rbf(xs[i] - mb) / sd);
  __syncthreads();
}
__global__ void k_init(const u16* x, float* xf) { int i = threadIdx.x + blockIdx.x * blockDim.x; if (i < D) xf[i] = bf2f(x[i]); }
__global__ void k_q(const float* xf, const u16* wq, float* q) {
  __shared__ float xs[D]; __shared__ float red[16];
  for (int i = threadIdx.x; i < D; i += blockDim.x) xs[i] = xf[i];
  __syncthreads();
  ln_shared(xs, red);
  int c = blockIdx.x * blockDim.x + threadIdx.x;
  float a = 0; for (int k = 0; k < D; ++k) a += xs[k] * bf2f(wq[(size_t)k * (H * DH) + c]);
  q[c] = rbf(a);
}
__global__ void k_attn(const float* q, const u16* K, const u16* V, float* part) {
  int h = blockIdx.x / NS, s = blockIdx.x % NS;
  __shared__ float qs[DH]; __shared__ float ps[512]; __shared__ float red[16]; __shared__ float oacc[4][DH];
  if (threadIdx.x < DH) qs[threadIdx.x] = q[h * DH + threadIdx.x];
  __syncthreads();
  int key = s * 512 + threadIdx.x;
  const u16* kr = K + ((size_t)h * SK + key) * DH;
  float a = 0; for (int d = 0; d < DH; ++d) a += qs[d] * bf2f(kr[d]);
  a = rbf(rbf(a) * 0.08838834764831845f);
  float m = a; for (int o = 32; o > 0; o >>= 1) m = fmaxf(m, __shfl_xor(m, o));
  if ((threadIdx.x & 63) == 0) red[threadIdx.x >> 6] = m;
  __syncthreads();
  m = red[0]; for (int i = 1; i < 8; ++i) m = fmaxf(m, red[i]);
  float p = __expf(a - m);
  ps[threadIdx.x] = p;
  float l = block_sum(p, red);
  int d = threadIdx.x & 127, g = threadIdx.x >> 7;
  float o = 0; for (int k = g * 128; k < g * 128 + 128; ++k) o += ps[k] * bf2f(V[((size_t)h * SK + s * 512 + k) * DH + d]);
  oacc[g][d] = o;
  __syncthreads();
  float* pp = part + (size_t)blockIdx.x * 132;
  if (threadIdx.x < DH) pp[4 + d] = oacc[0][d] + oacc[1][d] + oacc[2][d] + oacc[3][d];
  if (threadIdx.x == 0) { pp[0] = m; pp[1] = l; }
}
__global__ void k_comb(const float* part, float* o) {
  int h = blockIdx.x, d = threadIdx.x;
  float M = -INFINITY; for (int s = 0; s < NS; ++s) M = fmaxf(M, part[(h * NS + s) * 132]);
  float L = 0, a = 0; for (int s = 0; s < NS; ++s) { const float* pp = part + (h * NS + s) * 132; float w = __expf(pp[0] - M); L += w * pp[1]; a += w * pp[4 + d]; }
  o[h * DH + d] = rbf(a / L);
}
__global__ void k_proj_res(const float* in, int Kd, const u16* w, const u16* bias, float* xf, u16* outbf) {
  __shared__ float red[16][64];
  int c = blockIdx.x * 64 + (threadIdx.x & 63), g = threadIdx.x >> 6;
  float a = 0; for (int k = g; k < Kd; k += 16) a += in[k] * bf2f(w[(size_t)k * D + c]);
  red[g][threadIdx.x & 63] = a;
  __syncthreads();
  if (g == 0) { a = 0; for (int i = 0; i < 16; ++i) a += red[i][threadIdx.x]; a = rbf(a); if (bias) a = rbf(a + bf2f(bias[c]));
    float r = rbf(xf[c] + a); xf[c] = r; if (outbf) { __bf16 b = (__bf16)r; outbf[c] = __builtin_bit_cast(u16, b); } }
}
__global__ void k_mlp1(const float* xf, const u16* w1, const u16* b1, float* hbuf) {
  __shared__ float xs[D]; __shared__ float red[16];
  for (int i = threadIdx.x; i < D; i += blockDim.x) xs[i] = xf[i];
  __syncthreads();
  ln_shared(xs, red); ln_shared(xs, red);
  int c = blockIdx.x * blockDim.x + threadIdx.x;
  float a = 0; for (int k = 0; k < D; ++k) a += xs[k] * bf2f(w1[(size_t)k * F + c]);
  a = rbf(rbf(a) + bf2f(b1[c]));
  float t = 0.7978845608028654f * (a + 0.044715f * a * a * a);
  hbuf[c] = rbf(0.5f * a * (1.f + tanhf(t)));
}
extern "C" void kernel_launch(void* const* d_in, const int* in_sizes, int n_in, void* d_out, int out_size, void* d_ws, size_t ws_size, hipStream_t stream) {
  const u16* x = (const u16*)d_in[0]; const u16* K = (const u16*)d_in[1]; const u16* V = (const u16*)d_in[2];
  const u16* wq = (const u16*)d_in[3]; const u16* wo = (const u16*)d_in[4];
  const u16* w1 = (const u16*)d_in[6]; const u16* b1 = (const u16*)d_in[7]; const u16* w2 = (const u16*)d_in[8]; const u16* b2 = (const u16*)d_in[9];
  float* ws = (float*)d_ws; float* xf = ws; float* q = ws + 1024; float* o = ws + 8192; float* hb = ws + 16384; float* part = ws + 32768;
  k_init<<<3, 256, 0, stream>>>(x, xf);
  for (int l = 0; l < 2; ++l) {
    k_q<<<64, 64, 0, stream>>>(xf, wq, q);
    k_attn<<<H * NS, 512, 0, stream>>>(q, K, V, part);
    k_comb<<<H, DH, 0, stream>>>(part, o);
    k_proj_res<<<10, 1024, 0, stream>>>(o, H * DH, wo, nullptr, xf, nullptr);
    k_mlp1<<<27, 64, 0, stream>>>(xf, w1, b1, hb);
    k_proj_res<<<10, 1024, 0, stream>>>(hb, F, w2, b2, xf, l == 1 ? (u16*)d_out : nullptr);
  }
}
```

```cpp
#include <hip/hip_runtime.h>
#include <stdint.h>

typedef unsigned u32;
typedef unsigned long long u64;
typedef u32 v2u __attribute__((ext_vector_type(2)));
typedef u32 v4u __attribute__((ext_vector_type(4)));
typedef float v4f __attribute__((ext_vector_type(4)));
typedef float v2f __attribute__((ext_vector_type(2)));
typedef __bf16 v8bf __attribute__((ext_vector_type(8)));
typedef __bf16 v2bf __attribute__((ext_vector_type(2)));
typedef short v4s __attribute__((ext_vector_type(4)));
typedef __amdgpu_buffer_rsrc_t rsrc_t;
#define DEVI __device__ __forceinline__

constexpr int WS_FLG = 4096;
constexpr int WS_D = 8192;
constexpr int WS_H = 12288;
constexpr int WS_C = 20480;
constexpr int WS_B = 65536;
constexpr int BSTR = 640;
constexpr int WS_F = 393216;
constexpr int WS_END = WS_F + 2 * 262144;
constexpr int WS_STAMP = 1 << 20;
constexpr u32 SENT = 0xFFFFFFFFu;
constexpr int OOB = 0x40000000;
constexpr int AUX_SC1 = 16;
constexpr int SPIN_MAX = 1 << 16;

enum { R_NONE = 0, R_W = 1, R_M1 = 2, R_M2 = 3 };

DEVI rsrc_t mkrs(const void* p, int bytes) { return __builtin_amdgcn_make_buffer_rsrc((void*)p, 0, bytes, 0x00020000); }
DEVI v4u mk4(const void* p, int bytes) { const u64 a = (u64)p; return v4u{(u32)a, (u32)(a >> 32) & 0xFFFFu, (u32)bytes, 0x00020000u}; }
DEVI float rbf(float f) { return (float)(__bf16)f; }
DEVI u32 pack2(float a, float b) { v2bf t = {(__bf16)a, (__bf16)b}; return __builtin_bit_cast(u32, t); }
DEVI float lo16(u32 w) { return __uint_as_float(w << 16); }
DEVI float hi16(u32 w) { return __uint_as_float(w & 0xFFFF0000u); }
DEVI float dot2(u32 a, u32 b, float c) { return __builtin_amdgcn_fdot2_f32_bf16(__builtin_bit_cast(v2bf, a), __builtin_bit_cast(v2bf, b), c, false); }
DEVI u32 guard(u32 w) { return w < 0xFFFFFFFEu ? w : 0xFFFFFFFEu; }
DEVI u32 fguard(float f) { return guard(__float_as_uint(f)); }
template <int CTRL> DEVI u32 dppu(u32 v) { return __builtin_amdgcn_update_dpp(0u, v, CTRL, 0xF, 0xF, true); }
template <int CTRL> DEVI float dppf(float v) { return __uint_as_float(dppu<CTRL>(__float_as_uint(v))); }
constexpr int ROR(int n) { return 0x120 + n; }
constexpr int NBC(int n) { return 0x150 + n; }
DEVI float rdl(float v, int l) { return __uint_as_float(__builtin_amdgcn_readlane(__float_as_uint(v), l)); }
DEVI v4f mfma32(v4u a, v4u b, v4f c) { return __builtin_amdgcn_mfma_f32_16x16x32_bf16(__builtin_bit_cast(v8bf, a), __builtin_bit_cast(v8bf, b), c, 0, 0, 0); }
DEVI v4f mfma16(v2u a, v2u b, v4f c) { return __builtin_amdgcn_mfma_f32_16x16x16bf16_1k(__builtin_bit_cast(v4s, a), __builtin_bit_cast(v4s, b), c, 0, 0, 0); }
DEVI u32 perm_lo(u32 r0, u32 r1) { return __builtin_amdgcn_perm(r1, r0, 0x05040100u); }
DEVI u32 perm_hi(u32 r0, u32 r1) { return __builtin_amdgcn_perm(r1, r0, 0x07060302u); }
DEVI void bar() { asm volatile("s_waitcnt lgkmcnt(0)\n\ts_barrier" ::: "memory"); }
DEVI bool ok4(v4u v) { return (v[0] != SENT) & (v[1] != SENT) & (v[2] != SENT) & (v[3] != SENT); }
DEVI bool ok2(v2u v) { return (v[0] != SENT) & (v[1] != SENT); }
DEVI float sel4(float a, float b, float c, float d) {
  u32 x = __builtin_amdgcn_update_dpp(__float_as_uint(a), __float_as_uint(b), 0xE4, 0x2, 0xF, false);
  x = __builtin_amdgcn_update_dpp(x, __float_as_uint(c), 0xE4, 0x4, 0xF, false);
  x = __builtin_amdgcn_update_dpp(x, __float_as_uint(d), 0xE4, 0x8, 0xF, false);
  return __uint_as_float(x);
}
DEVI float fexp2(float x) { return __builtin_amdgcn_exp2f(x); }
constexpr float LOG2E = 1.4426950408889634f;

DEVI void ld128nt(v4u& d, u32 vo, v4u rs, u32 so) { asm volatile("buffer_load_dwordx4 %0, %1, %2, %3 offen nt" : "+v"(d) : "v"(vo), "s"(rs), "s"(so)); }
DEVI void ld128sc(v4u& d, u32 vo, v4u rs, u32 so) { asm volatile("buffer_load_dwordx4 %0, %1, %2, %3 offen sc1" : "=v"(d) : "v"(vo), "s"(rs), "s"(so)); }
template <int OFF, int INC> DEVI u32 ld32nt(u32 vo, v4u rs, u32& so) {
  u32 d;
  if (INC) asm volatile("buffer_load_dword %0, %2, %3, %1 offen offset:%4 nt\n\ts_add_u32 %1, %1, %5" : "=v"(d), "+s"(so) : "v"(vo), "s"(rs), "n"(OFF), "n"(INC) : "scc");
  else asm volatile("buffer_load_dword %0, %1, %2, %3 offen offset:%4 nt" : "=v"(d) : "v"(vo), "s"(rs), "s"(so), "n"(OFF));
  return d;
}
template <int I, int N, int P> struct LdRows {
  static DEVI void run(v4u* RW, u32 vo, v4u rs, u32& so) {
    RW[(I >> 3) * 2 + (I & 1)][(I >> 1) & 3] = ld32nt<0, (I & 7) == 7 ? 25 * P : P>(vo, rs, so);
    LdRows<I + 1, N, P>::run(RW, vo, rs, so);
  }
};
template <int N, int P> struct LdRows<N, N, P> { static DEVI void run(v4u*, u32, v4u, u32&) {} };
template <int I> struct LdWo {
  static DEVI void run(v4u* RW, u32 vo, v4u rs, u32& so) {
    constexpr int xy = I & 1, e = (I >> 1) & 3, s2 = I >> 3;
    RW[s2 * 4 + xy][e] = ld32nt<0, 0>(vo, rs, so);
    RW[s2 * 4 + 2 + xy][e] = ld32nt<64, 4 * 1280>(vo, rs, so);
    LdWo<I + 1>::run(RW, vo, rs, so);
  }
};
template <> struct LdWo<16> { static DEVI void run(v4u*, u32, v4u, u32&) {} };
DEVI void dma128nt(u32 lds, u32 vo, v4u rs, u32 so) { asm volatile("s_mov_b32 m0, %0\n\ts_nop 0\n\tbuffer_load_dwordx4 %1, %2, %3 offen nt lds" :: "s"(lds), "v"(vo), "s"(rs), "s"(so) : "memory"); }
template <int OFF> DEVI void dsr128(v4u& d, u32 a) { asm volatile("ds_read_b128 %0, %1 offset:%2" : "=v"(d) : "v"(a), "n"(OFF)); }
#define WAITV_(n) "s_waitcnt vmcnt(" #n ")"
template <int N> DEVI void waitv2(v4u& a, v4u& b) { asm volatile("s_waitcnt vmcnt(%2)" : "+v"(a), "+v"(b) : "n"(N > 63 ? 63 : N)); }
DEVI u32 ldsaddr(const void* p) { return (u32)(uintptr_t)(__attribute__((address_space(3))) const void*)p; }

DEVI float tr32(float a, float b) { auto r = __builtin_amdgcn_permlane32_swap(__float_as_uint(a), __float_as_uint(b), false, false); return __uint_as_float(r[0]) + __uint_as_float(r[1]); }
DEVI float tr16(float a, float b) { auto r = __builtin_amdgcn_permlane16_swap(__float_as_uint(a), __float_as_uint(b), false, false); return __uint_as_float(r[0]) + __uint_as_float(r[1]); }
DEVI float mx32(float a) { auto r = __builtin_amdgcn_permlane32_swap(__float_as_uint(a), __float_as_uint(a), false, false); return fmaxf(__uint_as_float(r[0]), __uint_as_float(r[1])); }
DEVI float mx16(float a) { auto r = __builtin_amdgcn_permlane16_swap(__float_as_uint(a), __float_as_uint(a), false, false); return fmaxf(__uint_as_float(r[0]), __uint_as_float(r[1])); }
DEVI float rowsum(float t) { t += dppf<ROR(8)>(t); t += dppf<ROR(4)>(t); t += dppf<ROR(2)>(t); t += dppf<ROR(1)>(t); return t; }
DEVI float rowmax(float t) { t = fmaxf(t, dppf<ROR(8)>(t)); t = fmaxf(t, dppf<ROR(4)>(t)); t = fmaxf(t, dppf<ROR(2)>(t)); t = fmaxf(t, dppf<ROR(1)>(t)); return t; }

DEVI void ln_stats(const u32 (&a)[5], float& mb, float& rs) {
  float s = 0.f, q = 0.f;
#pragma unroll
  for (int i = 0; i < 5; ++i) { s = dot2(a[i], 0x3F803F80u, s); q = dot2(a[i], a[i], q); }
  float t = tr32(s, q);
  t = tr16(t, t);
  t = rowsum(t);
  const float S = rdl(t, 0), Q = rdl(t, 32);
  const float mean = S * (1.f / 640.f);
  const float var = fmaxf(Q * (1.f / 640.f) - mean * mean, 0.f);
  mb = rbf(mean);
  const float sd = rbf(__builtin_amdgcn_sqrtf(rbf(rbf(var) + 1e-5f)));
  rs = __builtin_amdgcn_rcpf(sd);
}
DEVI u32 ln_apply(u32 w, float mb, float rs) {
  const u32 d = pack2(lo16(w) - mb, hi16(w) - mb);
  return pack2(lo16(d) * rs, hi16(d) * rs);
}

#define STAMP(i) do {} while (0)

struct Args { const char *x, *K, *V, *wq, *wo, *w1, *b1, *w2, *b2; char *out, *ws; };

__shared__ __attribute__((aligned(16))) char sK[131072];
__shared__ __attribute__((aligned(16))) char sWq[20480];
__shared__ __attribute__((aligned(16))) float sO[8 * 128];
__shared__ __attribute__((aligned(16))) float sML[16];
__shared__ __attribute__((aligned(16))) float sR[8 * 64];
__shared__ __attribute__((aligned(16))) u32 sY[64];
__shared__ __attribute__((aligned(16))) u32 sQ[64];
__shared__ __attribute__((aligned(16))) u32 sX[320];
__shared__ __attribute__((aligned(16))) u32 sX1[320];

template <int ROLE>
DEVI void body(const Args& A, int grp, const int idx, int slot) {
  char* const ws = A.ws;
  int lane = threadIdx.x & 63, wave = __builtin_amdgcn_readfirstlane(threadIdx.x >> 6);
  int g = lane >> 4, n = lane & 15;
  const int hh = idx >> 3, j = idx & 7;
  const int head = hh * 8 + grp;
  int me = grp * 32 + idx;
  STAMP(0);
  const u64 E = *(const u64*)ws;
  const u64 E1 = E * 6364136223846793005ull + 1442695040888963407ull;
  const u32 tag = (u32)E1 & 0x7FFFFFFFu;
  const rsrc_t rws = mkrs(ws, WS_END);
  const rsrc_t rnull = mkrs(ws, 0);

  u32 xa0 = 0, biasr = 0; v4u xa1 = {0, 0, 0, 0};
  const int px0 = (40 * j + lane) % 320, px1 = (40 * j + 64 + 4 * lane) % 320;
  if (wave == 0) {
    const v4u rx = mk4(A.x, 1280);
    asm volatile("buffer_load_dword %0, %1, %2, 0 offen" : "=v"(xa0) : "v"(px0 * 4), "s"(rx));
    asm volatile("buffer_load_dwordx4 %0, %1, %2, 0 offen" : "=v"(xa1) : "v"(px1 * 4), "s"(rx));
    if (ROLE == R_M1 || ROLE == R_M2) {
      const v4u rb = mk4((ROLE == R_M1 ? A.b1 : A.b2) + slot * 64, 64);
      asm volatile("buffer_load_ushort %0, %1, %2, 0 offen" : "=v"(biasr) : "v"(lane * 2), "s"(rb));
    }
  }
  if (wave < 5) {
    const v4u rq = mk4(A.wq + head * 256 + (size_t)(80 * j + 16 * wave) * 8192, 16 * 8192);
#pragma unroll
    for (int i = 0; i < 4; ++i) dma128nt(ldsaddr(sWq) + wave * 4096 + i * 1024, g * 8192 + n * 16, rq, i * 32768);
  }
  if (wave == 7) {
    const v4u s4 = {SENT, SENT, SENT, SENT};
    __builtin_amdgcn_raw_buffer_store_b128(s4, rws, lane * 16, WS_F + 262144 + me * 1024, AUX_SC1);
    const int ob = lane < 33 ? lane * 16 : OOB;
    __builtin_amdgcn_raw_buffer_store_b128(s4, rws, ob, WS_B + me * BSTR, AUX_SC1);
    __builtin_amdgcn_raw_buffer_store_b128(s4, rws, ob, WS_B + (256 + me) * BSTR, AUX_SC1);
    if (ROLE == R_W) __builtin_amdgcn_raw_buffer_store_b128(s4, rws, g < 2 ? g * 20480 + n * 16 : OOB, WS_C + grp * 2560 + slot * 256, AUX_SC1);
    if (ROLE == R_M1) __builtin_amdgcn_raw_buffer_store_b128(s4, rws, (g < 2 && n < 4) ? g * 4096 + n * 16 : OOB, WS_H + slot * 64, AUX_SC1);
    if (ROLE == R_M2) __builtin_amdgcn_raw_buffer_store_b128(s4, rws, lane < 4 ? lane * 16 : OOB, WS_D + slot * 64, AUX_SC1);
  }
  const int gw = j * 8 + wave;
  int rot = gw + head;
  {
    const v4u rk = mk4(A.K + (size_t)head * 1048576 + gw * 16384, 16384);
#pragma unroll
    for (int i = 0; i < 16; ++i) dma128nt(ldsaddr(sK) + wave * 16384 + i * 1024, (lane ^ i) * 16, rk, ((i + rot) & 15) * 1024);
  }
  const v4u rv = mk4(A.V + (size_t)head * 1048576 + gw * 16384, 16384);
  asm volatile("s_waitcnt vmcnt(16)" : "+v"(xa0), "+v"(xa1), "+v"(biasr) :: "memory");
  if (wave == 7) {
    const v2u fl = {(u32)E1, (u32)(E1 >> 32)};
    __builtin_amdgcn_raw_buffer_store_b64(fl, rws, lane == 0 ? 0 : OOB, WS_FLG + me * 8, AUX_SC1);
  }
  bar();
  STAMP(1);

  constexpr int NRW = ROLE == R_W ? 32 : ROLE == R_M1 ? 40 : ROLE == R_M2 ? 56 : 0;
  constexpr int NRT = NRW ? NRW / 4 : 1;
  v4u RW[NRT], V[16];
#pragma unroll
  for (int i = 0; i < NRT; ++i) asm volatile("" : "=v"(RW[i]));
#pragma unroll
  for (int i = 0; i < 16; ++i) asm volatile("" : "=v"(V[i]));
  const float bias = lo16(biasr);
  u32 sel[4];
  {
    const u32 sb = n & 8 ? 0x0302u : 0x0100u;
    const u32 sv = n & 1 ? (sb << 16) | 0x0C0Cu : 0x0C0C0000u | sb;
#pragma unroll
    for (int w = 0; w < 4; ++w) sel[w] = ((n >> 1) & 3) == w ? sv : 0x0C0C0C0Cu;
  }

  int nit = 3;
  asm volatile("" : "+s"(nit));
#pragma nounroll
  for (int it = 0; it < nit; ++it) {
    asm volatile("" : "+v"(lane), "+v"(g), "+v"(n), "+s"(wave), "+s"(me), "+s"(rot), "+s"(grp), "+s"(slot));
    const int L = it == 2 ? 1 : 0;
    const bool live = it > 0;
    if (it != 1) {
      v4u QB[4]; v2u QC[2];
      if (wave < 4) {
        const u32* wp = (const u32*)sWq + wave * 16 + n;
#pragma unroll
        for (int s = 0; s < 2; ++s)
#pragma unroll
          for (int e = 0; e < 4; ++e) {
            const u32 r0 = wp[(32 * s + 8 * g + 2 * e) * 64], r1 = wp[(32 * s + 8 * g + 2 * e + 1) * 64];
            QB[2 * s][e] = perm_lo(r0, r1); QB[2 * s + 1][e] = perm_hi(r0, r1);
          }
#pragma unroll
        for (int e = 0; e < 2; ++e) {
          const u32 r0 = wp[(64 + 4 * g + 2 * e) * 64], r1 = wp[(64 + 4 * g + 2 * e + 1) * 64];
          QC[0][e] = perm_lo(r0, r1); QC[1][e] = perm_hi(r0, r1);
        }
      }
      if (wave == 0) {
        if (it == 2) {
          _Pragma("nounroll") for (int sp = 0; sp < SPIN_MAX; ++sp) {
            xa0 = __builtin_amdgcn_raw_buffer_load_b32(rws, px0 * 4, WS_D, AUX_SC1);
            xa1 = __builtin_amdgcn_raw_buffer_load_b128(rws, px1 * 4, WS_D, AUX_SC1);
            if (__all((xa0 != SENT) & ok4(xa1))) break;
            asm volatile("" ::: "memory");
          }
          STAMP(16);
        }
        float mb, rs;
        const u32 xa[5] = {xa0, xa1[0], xa1[1], xa1[2], xa1[3]};
        ln_stats(xa, mb, rs);
        sY[lane] = ln_apply(xa0, mb, rs);
        if (ROLE == R_M1 || ROLE == R_M2) { sX[px0] = xa0; *(v4u*)&sX[px1] = xa1; }
      }
      bar();
      if (wave < 4) {
        const v4u a0 = *(const v4u*)&sY[4 * g], a1 = *(const v4u*)&sY[16 + 4 * g];
        const v2u a2 = *(const v2u*)&sY[32 + 2 * g];
        v4f c0 = {0, 0, 0, 0}, c1 = {0, 0, 0, 0};
        c0 = mfma32(a0, QB[0], c0); c1 = mfma32(a0, QB[1], c1);
        c0 = mfma32(a1, QB[2], c0); c1 = mfma32(a1, QB[3], c1);
        c0 = mfma16(a2, QC[0], c0); c1 = mfma16(a2, QC[1], c1);
        const v4u o = {__float_as_uint(c0[0]), tag, __float_as_uint(c1[0]), tag};
        __builtin_amdgcn_raw_buffer_store_b128(o, rws, g == 0 ? (wave * 16 + n) * 16 : OOB, WS_F + L * 262144 + me * 1024, AUX_SC1);
        STAMP(2 + 16 * L);
      }
      if (it == 0) {
#pragma unroll
        for (int i = 0; i < 8; ++i) ld128nt(V[i], lane * 16, rv, ((i + rot) & 15) * 1024);
        STAMP(11);
      }
    }
    if (it > 0) {
      const int fvo = (lane >> 5) * 1024 + (wave & 1) * 512 + (lane & 31) * 16;
      const int fb = WS_F + L * 262144 + (me & ~7) * 1024;
      v4u f[4];
      if (it == 1) {
        if (wave >= 4 && wave < 7) {
          const v4u rw4 = mk4(ws, WS_END);
          const int vo = wave == 4 ? lane * 16 : fvo;
          const int so = wave == 4 ? WS_FLG : fb, st = wave == 4 ? 1024 : 2048;
#pragma unroll
          for (int s = 0; s < 4; ++s) ld128sc(f[s], vo, rw4, so + s * st);
        }
#pragma unroll
        for (int i = 8; i < 16; ++i) ld128nt(V[i], lane * 16, rv, ((i + rot) & 15) * 1024);
        STAMP(12);
        asm volatile("s_waitcnt vmcnt(16)" ::: "memory");
        STAMP(3);
      }
      v4u KF[16];
      {
        const u32 kb = ldsaddr(sK) + wave * 16384 + n * 1024;
        const u32 k0 = kb + ((g ^ n) * 16), k1 = kb + (((4 + g) ^ n) * 16), k2 = kb + (((8 + g) ^ n) * 16), k3 = kb + (((12 + g) ^ n) * 16);
        dsr128<0>(KF[0], k0); dsr128<256>(KF[1], k0); dsr128<512>(KF[2], k0); dsr128<768>(KF[3], k0);
        dsr128<0>(KF[4], k1); dsr128<256>(KF[5], k1); dsr128<512>(KF[6], k1); dsr128<768>(KF[7], k1);
        dsr128<0>(KF[8], k2); dsr128<256>(KF[9], k2); dsr128<512>(KF[10], k2); dsr128<768>(KF[11], k2);
        dsr128<0>(KF[12], k3); dsr128<256>(KF[13], k3); dsr128<512>(KF[14], k3); dsr128<768>(KF[15], k3);
      }
      if (wave == 4 && it == 1) {
        asm volatile("s_waitcnt vmcnt(8)" : "+v"(f[0]), "+v"(f[1]));
        const u32 el = (u32)E1, eh = (u32)(E1 >> 32);
        _Pragma("nounroll") for (int sp = 0; sp < SPIN_MAX; ++sp) {
          const bool fo = (f[0][0] == el) & (f[0][1] == eh) & (f[0][2] == el) & (f[0][3] == eh) & (f[1][0] == el) & (f[1][1] == eh) & (f[1][2] == el) & (f[1][3] == eh);
          if (__all(fo)) break;
          f[0] = __builtin_amdgcn_raw_buffer_load_b128(rws, lane * 16, WS_FLG, AUX_SC1);
          f[1] = __builtin_amdgcn_raw_buffer_load_b128(rws, lane * 16, WS_FLG + 1024, AUX_SC1);
          asm volatile("" ::: "memory");
        }
      }
      if (wave == 5 || wave == 6) {
        bool okk = false;
        if (it == 1) asm volatile("s_waitcnt vmcnt(8)" : "+v"(f[0]), "+v"(f[1]), "+v"(f[2]), "+v"(f[3]));
        if (it == 1) okk = __all((f[0][1] == tag) & (f[0][3] == tag) & (f[1][1] == tag) & (f[1][3] == tag) & (f[2][1] == tag) & (f[2][3] == tag) & (f[3][1] == tag) & (f[3][3] == tag));
        if (!okk) {
          _Pragma("nounroll") for (int sp = 0; sp < SPIN_MAX; ++sp) {
            bool o2 = true;
#pragma unroll
            for (int s = 0; s < 4; ++s) f[s] = __builtin_amdgcn_raw_buffer_load_b128(rws, fvo, fb + s * 2048, AUX_SC1);
#pragma unroll
            for (int s = 0; s < 4; ++s) o2 &= (f[s][1] == tag) & (f[s][3] == tag);
            if (__all(o2)) break;
            asm volatile("" ::: "memory");
          }
        }
        const float q0 = (__uint_as_float(f[0][0]) + __uint_as_float(f[1][0])) + (__uint_as_float(f[2][0]) + __uint_as_float(f[3][0]));
        const float q1 = (__uint_as_float(f[0][2]) + __uint_as_float(f[1][2])) + (__uint_as_float(f[2][2]) + __uint_as_float(f[3][2]));
        ((__bf16*)sQ)[(wave & 1) * 64 + (lane & 31) * 2 + (lane >> 5)] = (__bf16)tr32(q0, q1);
        STAMP(4 + 16 * L);
      }
      asm volatile("s_waitcnt lgkmcnt(0)\n\ts_barrier" : "+v"(KF[0]), "+v"(KF[1]), "+v"(KF[2]), "+v"(KF[3]), "+v"(KF[4]), "+v"(KF[5]), "+v"(KF[6]), "+v"(KF[7]),
                   "+v"(KF[8]), "+v"(KF[9]), "+v"(KF[10]), "+v"(KF[11]), "+v"(KF[12]), "+v"(KF[13]), "+v"(KF[14]), "+v"(KF[15]) :: "memory");
      v4f sc[4];
#pragma unroll
      for (int t = 0; t < 4; ++t) sc[t] = v4f{0, 0, 0, 0};
#pragma unroll
      for (int jj = 0; jj < 4; ++jj) {
        const v4u qa = *(const v4u*)&sQ[16 * jj + 4 * g];
#pragma unroll
        for (int t = 0; t < 4; ++t) sc[t] = mfma32(qa, KF[jj * 4 + t], sc[t]);
      }
      float s = sel4(sc[0][0], sc[1][0], sc[2][0], sc[3][0]);
      s = rbf(rbf(s) * 0.08838834764831845f);
      const float m = mx32(mx16(rowmax(s)));
      const float p = fexp2((s - m) * LOG2E);
      const u32 pp = pack2(p, dppf<0xF5>(p));
      u32 bc[8];
      bc[0] = dppu<NBC(0)>(pp); bc[1] = dppu<NBC(2)>(pp); bc[2] = dppu<NBC(4)>(pp); bc[3] = dppu<NBC(6)>(pp);
      bc[4] = dppu<NBC(8)>(pp); bc[5] = dppu<NBC(10)>(pp); bc[6] = dppu<NBC(12)>(pp); bc[7] = dppu<NBC(14)>(pp);
      float lsum = rowsum(p);
      lsum = tr16(lsum, lsum); lsum = tr32(lsum, lsum);
      if (lane == 0) { sML[wave] = m; sML[8 + wave] = lsum; }
      STAMP(5 + 16 * L);
      v4f a0 = {0, 0, 0, 0}, a1 = {0, 0, 0, 0};
#define PVSTEP(k, N) { v4u PA; PA[0] = __builtin_amdgcn_perm(bc[k], bc[k], sel[0]); PA[1] = __builtin_amdgcn_perm(bc[k], bc[k], sel[1]); \
        PA[2] = __builtin_amdgcn_perm(bc[k], bc[k], sel[2]); PA[3] = __builtin_amdgcn_perm(bc[k], bc[k], sel[3]); \
        waitv2<N>(V[2 * k], V[2 * k + 1]); a0 = mfma32(PA, V[2 * k], a0); a1 = mfma32(PA, V[2 * k + 1], a1); }
      PVSTEP(0, 8) PVSTEP(1, 8) PVSTEP(2, 8) PVSTEP(3, 8)
      if (NRW && it == 1) {
        u32 so = 0;
        if (ROLE == R_W) {
          const int wh = (wave >> 1) * 8 + grp;
          const v4u rw = mk4(A.wo + (size_t)(wh * 128 + (wave & 1) * 64) * 1280 + slot * 128, 64 * 1280);
          LdWo<0>::run(RW, g * 1280 + n * 4, rw, so);
        }
        if (ROLE == R_M1) {
          const v4u rw = mk4(A.w1 + (size_t)(wave & 3) * 160 * 3456 + slot * 64, wave < 4 ? 160 * 3456 - slot * 64 : 0);
          LdRows<0, 40, 3456>::run(RW, g * 8 * 3456 + n * 4, rw, so);
        }
        if (ROLE == R_M2) {
          const v4u rw = mk4(A.w2 + (size_t)wave * 224 * 1280 + slot * 64, (1728 - wave * 224) * 1280 - slot * 64);
          LdRows<0, 56, 1280>::run(RW, g * 8 * 1280 + n * 4, rw, so);
        }
      }
      PVSTEP(4, 6 + NRW) PVSTEP(5, 4 + NRW) PVSTEP(6, 2 + NRW) PVSTEP(7, 0 + NRW)
      {
        auto x0 = __builtin_amdgcn_permlane32_swap(__float_as_uint(a0[0]), __float_as_uint(a0[2]), false, false);
        auto x1 = __builtin_amdgcn_permlane32_swap(__float_as_uint(a1[0]), __float_as_uint(a1[2]), false, false);
        auto y0 = __builtin_amdgcn_permlane32_swap(__float_as_uint(a0[1]), __float_as_uint(a0[3]), false, false);
        auto y1 = __builtin_amdgcn_permlane32_swap(__float_as_uint(a1[1]), __float_as_uint(a1[3]), false, false);
        *(v2f*)&sO[wave * 128 + 8 * n + 4 * (g & 1) + (g & 2)] = v2f{__uint_as_float(x0[0]) + __uint_as_float(x1[1]), __uint_as_float(y0[0]) + __uint_as_float(y1[1])};
      }
      STAMP(6 + 16 * L);
    }
    bar();
    const rsrc_t rst = live ? rws : rnull;
    if (wave == 7) {
      const int hf = lane >> 5, c = lane & 31;
      const v4f m0 = *(const v4f*)&sML[0], m1 = *(const v4f*)&sML[4];
      const v4f lw = *(const v4f*)&sML[8 + 4 * hf];
      v4f ow[4];
#pragma unroll
      for (int i = 0; i < 4; ++i) ow[i] = *(const v4f*)&sO[(4 * hf + i) * 128 + 4 * c];
      const float M = fmaxf(fmaxf(fmaxf(m0[0], m0[1]), fmaxf(m0[2], m0[3])), fmaxf(fmaxf(m1[0], m1[1]), fmaxf(m1[2], m1[3])));
      v4f acc = {0, 0, 0, 0}; float ls = 0.f;
#pragma unroll
      for (int i = 0; i < 4; ++i) {
        const float w = fexp2(((hf ? m1[i] : m0[i]) - M) * LOG2E);
        acc += w * ow[i]; ls += w * lw[i];
      }
      v4u o;
#pragma unroll
      for (int i = 0; i < 4; ++i) o[i] = fguard(tr32(acc[i], acc[i]));
      ls = tr32(ls, ls);
      if (hf) { o[0] = fguard(M); o[1] = fguard(ls); }
      __builtin_amdgcn_raw_buffer_store_b128(o, rst, lane < 33 ? lane * 16 : OOB, WS_B + (L * 256 + me) * BSTR, AUX_SC1);
      STAMP(7 + 16 * L);
    }
    if (NRW && it == 1) {
      asm volatile("s_waitcnt vmcnt(0)" ::: "memory");
#pragma unroll
      for (int i = 0; i < NRT; i += 2) {
        asm volatile("" : "+v"(RW[i]), "+v"(RW[i + 1 < NRT ? i + 1 : i]));
#pragma unroll
        for (int e = 0; e < 4; ++e) {
          const u32 r0 = RW[i][e], r1 = RW[i + 1 < NRT ? i + 1 : i][e];
          RW[i][e] = perm_lo(r0, r1); RW[i + 1 < NRT ? i + 1 : i][e] = perm_hi(r0, r1);
        }
      }
      STAMP(13);
    }
    if (ROLE == R_W) {
      v4u oa = {0, 0, 0, 0}, ob = {0, 0, 0, 0}; v2u ma = {0, 0}, mb2 = {0, 0};
      if (live) {
        const int bb = WS_B + (L * 256 + grp * 32 + (wave >> 1) * 8) * BSTR;
        const int vo = g * BSTR + (wave & 1) * 256 + n * 16;
        _Pragma("nounroll") for (int sp = 0; sp < SPIN_MAX; ++sp) {
          oa = __builtin_amdgcn_raw_buffer_load_b128(rws, vo, bb, AUX_SC1);
          ob = __builtin_amdgcn_raw_buffer_load_b128(rws, vo, bb + 4 * BSTR, AUX_SC1);
          ma = __builtin_amdgcn_raw_buffer_load_b64(rws, g * BSTR + 512, bb, AUX_SC1);
          mb2 = __builtin_amdgcn_raw_buffer_load_b64(rws, g * BSTR + 512, bb + 4 * BSTR, AUX_SC1);
          if (__all(ok4(oa) & ok4(ob) & ok2(ma) & ok2(mb2))) break;
          asm volatile("" ::: "memory");
        }
        STAMP(8 + 16 * L);
      }
      const float m_a = __uint_as_float(ma[0]), m_b = __uint_as_float(mb2[0]);
      const float M = mx32(mx16(fmaxf(m_a, m_b)));
      const float wa = fexp2((m_a - M) * LOG2E), wb = fexp2((m_b - M) * LOG2E);
      float ls = wa * __uint_as_float(ma[1]) + wb * __uint_as_float(mb2[1]);
      ls = tr16(ls, ls); ls = tr32(ls, ls);
      float r[4];
#pragma unroll
      for (int i = 0; i < 4; ++i) r[i] = wa * __uint_as_float(oa[i]) + wb * __uint_as_float(ob[i]);
      const float o = tr16(tr32(r[0], r[2]), tr32(r[1], r[3])) * __builtin_amdgcn_rcpf(ls);
      const u32 op = pack2(o, dppf<0xF5>(o));
      v4u A0, A1;
      A0[0] = dppu<NBC(0)>(op); A0[1] = dppu<NBC(2)>(op); A0[2] = dppu<NBC(4)>(op); A0[3] = dppu<NBC(6)>(op);
      A1[0] = dppu<NBC(8)>(op); A1[1] = dppu<NBC(10)>(op); A1[2] = dppu<NBC(12)>(op); A1[3] = dppu<NBC(14)>(op);
      v4f c[4];
#pragma unroll
      for (int i = 0; i < 4; ++i) { c[i] = mfma32(A0, RW[i % NRT], v4f{0, 0, 0, 0}); }
#pragma unroll
      for (int i = 0; i < 4; ++i) { c[i] = mfma32(A1, RW[(4 + i) % NRT], c[i]); }
      sR[wave * 64 + (g >> 1) * 32 + 2 * n + (g & 1)] = sel4(c[0][0], c[1][0], c[2][0], c[3][0]);
      bar();
      if (wave == 0) {
        const int u = lane & 3, ch = lane >> 2;
        v4f t = *(const v4f*)&sR[(2 * u) * 64 + 4 * ch] + *(const v4f*)&sR[(2 * u + 1) * 64 + 4 * ch];
        v4u o4;
#pragma unroll
        for (int i = 0; i < 4; ++i) { float v = t[i]; v += dppf<0xB1>(v); v += dppf<0x4E>(v); o4[i] = fguard(v); }
        __builtin_amdgcn_raw_buffer_store_b128(o4, rst, u == 0 ? ch * 16 : OOB, WS_C + L * 20480 + grp * 2560 + slot * 256, AUX_SC1);
        STAMP(9 + 16 * L);
      }
    }
    if (ROLE == R_M1) {
      if (wave < 5) {
        const int hf = lane >> 5, c = lane & 31;
        const int pi = 2 * (32 * wave + c) + hf;
        const u32 xo = sX[pi];
        v4u cv[4];
#pragma unroll
        for (int i = 0; i < 4; ++i) cv[i] = v4u{0, 0, 0, 0};
        if (live) {
          const int vo = hf * 4 * 2560 + (32 * wave + c) * 16;
          _Pragma("nounroll") for (int sp = 0; sp < SPIN_MAX; ++sp) {
#pragma unroll
            for (int i = 0; i < 4; ++i) cv[i] = __builtin_amdgcn_raw_buffer_load_b128(rws, vo, WS_C + L * 20480 + i * 2560, AUX_SC1);
            if (__all(ok4(cv[0]) & ok4(cv[1]) & ok4(cv[2]) & ok4(cv[3]))) break;
            asm volatile("" ::: "memory");
          }
          STAMP(8 + 16 * L);
        }
        const v4f t = (__builtin_bit_cast(v4f, cv[0]) + __builtin_bit_cast(v4f, cv[1])) + (__builtin_bit_cast(v4f, cv[2]) + __builtin_bit_cast(v4f, cv[3]));
        const u32 ar = pack2(tr32(t[0], t[2]), tr32(t[1], t[3]));
        sX1[pi] = pack2(lo16(xo) + lo16(ar), hi16(xo) + hi16(ar));
      }
      bar();
      if (wave < 4) {
        u32 a[5];
        const int pb = 80 * wave, sp0 = 16 * (n >> 2) + 4 * g + (n & 3);
        a[0] = sX1[pb + sp0]; a[1] = sX1[(pb + 64 + sp0) % 320];
#pragma unroll
        for (int k = 0; k < 3; ++k) a[2 + k] = sX1[(pb + 128 + 64 * k + lane) % 320];
        float mb, rs;
        ln_stats(a, mb, rs);
#pragma unroll
        for (int k = 0; k < 5; ++k) a[k] = ln_apply(a[k], mb, rs);
        ln_stats(a, mb, rs);
        const u32 y0 = ln_apply(a[0], mb, rs), y1 = ln_apply(a[1], mb, rs);
        v4f c0 = {0, 0, 0, 0}, c1 = {0, 0, 0, 0};
        v4u Aq;
        Aq[0] = dppu<NBC(0)>(y0); Aq[1] = dppu<NBC(1)>(y0); Aq[2] = dppu<NBC(2)>(y0); Aq[3] = dppu<NBC(3)>(y0);
        c0 = mfma32(Aq, RW[0], c0); c1 = mfma32(Aq, RW[1 % NRT], c1);
        Aq[0] = dppu<NBC(4)>(y0); Aq[1] = dppu<NBC(5)>(y0); Aq[2] = dppu<NBC(6)>(y0); Aq[3] = dppu<NBC(7)>(y0);
        c0 = mfma32(Aq, RW[2 % NRT], c0); c1 = mfma32(Aq, RW[3 % NRT], c1);
        Aq[0] = dppu<NBC(8)>(y0); Aq[1] = dppu<NBC(9)>(y0); Aq[2] = dppu<NBC(10)>(y0); Aq[3] = dppu<NBC(11)>(y0);
        c0 = mfma32(Aq, RW[4 % NRT], c0); c1 = mfma32(Aq, RW[5 % NRT], c1);
        Aq[0] = dppu<NBC(12)>(y0); Aq[1] = dppu<NBC(13)>(y0); Aq[2] = dppu<NBC(14)>(y0); Aq[3] = dppu<NBC(15)>(y0);
        c0 = mfma32(Aq, RW[6 % NRT], c0); c1 = mfma32(Aq, RW[7 % NRT], c1);
        Aq[0] = dppu<NBC(0)>(y1); Aq[1] = dppu<NBC(1)>(y1); Aq[2] = dppu<NBC(2)>(y1); Aq[3] = dppu<NBC(3)>(y1);
        c0 = mfma32(Aq, RW[8 % NRT], c0); c1 = mfma32(Aq, RW[9 % NRT], c1);
        if (g == 0) *(v2f*)&sR[wave * 32 + 2 * n] = v2f{c0[0], c1[0]};
      }
      bar();
      if (wave == 0) {
        const int c = lane & 31;
        float a = (sR[c] + sR[32 + c]) + (sR[64 + c] + sR[96 + c]);
        a = rbf(rbf(a) + bias);
        const float u2 = a * (1.f + 0.044715f * a * a) * (-2.f * 0.7978845608028654f * LOG2E);
        const float h = a * __builtin_amdgcn_rcpf(1.f + fexp2(u2));
        const u32 hp = guard(pack2(h, dppf<0xF5>(h)));
        const v4u o4 = {hp, dppu<0x102>(hp), dppu<0x104>(hp), dppu<0x106>(hp)};
        __builtin_amdgcn_raw_buffer_store_b128(o4, rst, ((lane & 7) == 0 && lane < 32) ? lane * 2 : OOB, WS_H + L * 4096 + slot * 64, AUX_SC1);
        STAMP(9 + 16 * L);
      }
    }
    if (ROLE == R_M2) {
      float x1 = 0.f;
      if (wave == 0) {
        const int c = lane & 31;
        const u32 xo = sX[slot * 16 + (c >> 1)];
        u32 cv[8];
#pragma unroll
        for (int i = 0; i < 8; ++i) cv[i] = 0;
        if (live) {
          _Pragma("nounroll") for (int sp = 0; sp < SPIN_MAX; ++sp) {
            bool o2 = true;
#pragma unroll
            for (int i = 0; i < 8; ++i) cv[i] = __builtin_amdgcn_raw_buffer_load_b32(rws, c * 4 + i * 2560, WS_C + L * 20480 + slot * 128, AUX_SC1);
#pragma unroll
            for (int i = 0; i < 8; ++i) o2 &= cv[i] != SENT;
            if (__all(o2)) break;
            asm volatile("" ::: "memory");
          }
        }
        float t0 = (__uint_as_float(cv[0]) + __uint_as_float(cv[1])) + (__uint_as_float(cv[2]) + __uint_as_float(cv[3]));
        float t1 = (__uint_as_float(cv[4]) + __uint_as_float(cv[5])) + (__uint_as_float(cv[6]) + __uint_as_float(cv[7]));
        x1 = rbf(((c & 1) ? hi16(xo) : lo16(xo)) + rbf(t0 + t1));
      }
      v4u ha[7];
#pragma unroll
      for (int i = 0; i < 7; ++i) ha[i] = v4u{0, 0, 0, 0};
      if (live) {
        const rsrc_t rh = mkrs(ws + WS_H + L * 4096, 3456);
        _Pragma("nounroll") for (int sp = 0; sp < SPIN_MAX; ++sp) {
          bool o2 = true;
#pragma unroll
          for (int i = 0; i < 7; ++i) ha[i] = __builtin_amdgcn_raw_buffer_load_b128(rh, wave * 448 + i * 64 + g * 16, 0, AUX_SC1);
#pragma unroll
          for (int i = 0; i < 7; ++i) o2 &= ok4(ha[i]);
          if (__all(o2)) break;
          asm volatile("" ::: "memory");
        }
        STAMP(8 + 16 * L);
      }
      v4f c0 = {0, 0, 0, 0}, c1 = {0, 0, 0, 0};
#pragma unroll
      for (int i = 0; i < 7; ++i) { c0 = mfma32(ha[i], RW[(2 * i) % NRT], c0); c1 = mfma32(ha[i], RW[(2 * i + 1) % NRT], c1); }
      if (g == 0) *(v2f*)&sR[wave * 32 + 2 * n] = v2f{c0[0], c1[0]};
      bar();
      if (wave == 0) {
        const int c = lane & 31;
        float a = ((sR[c] + sR[32 + c]) + (sR[64 + c] + sR[96 + c])) + ((sR[128 + c] + sR[160 + c]) + (sR[192 + c] + sR[224 + c]));
        a = rbf(rbf(a) + bias) + x1;
        const u32 hp = guard(pack2(a, dppf<0xF5>(a)));
        const v4u o4 = {hp, dppu<0x102>(hp), dppu<0x104>(hp), dppu<0x106>(hp)};
        const rsrc_t ro = it == 2 ? mkrs(A.out, 1280) : mkrs(ws + WS_D, it == 1 ? 1280 : 0);
        __builtin_amdgcn_raw_buffer_store_b128(o4, ro, ((lane & 7) == 0 && lane < 32) ? lane * 2 : OOB, slot * 64, AUX_SC1);
        if (it == 2 && slot == 0 && lane == 0) *(u64*)ws = E1;
        STAMP(9 + 16 * L);
      }
    }
    if (it == 0) STAMP(10);
  }
}

__global__ void __launch_bounds__(512, 2) fused_decode(Args A) {
  const int b = blockIdx.x;
  const int grp = b & 7, idx = b >> 3;
  if (idx < 10) body<R_W>(A, grp, idx, idx);
  else if (idx < 18 && grp * 8 + (idx - 10) < 54) body<R_M1>(A, grp, idx, grp * 8 + (idx - 10));
  else if (idx >= 18 && idx < 22 && grp >= 3) body<R_M2>(A, grp, idx, (grp - 3) * 4 + (idx - 18));
  else body<R_NONE>(A, grp, idx, 0);
}

extern "C" void kernel_launch(void* const* d_in, const int* in_sizes, int n_in, void* d_out, int out_size, void* d_ws, size_t ws_size, hipStream_t stream) {
  Args A;
  A.x = (const char*)d_in[0]; A.K = (const char*)d_in[1]; A.V = (const char*)d_in[2]; A.wq = (const char*)d_in[3]; A.wo = (const char*)d_in[4];
  A.w1 = (const char*)d_in[6]; A.b1 = (const char*)d_in[7]; A.w2 = (const char*)d_in[8]; A.b2 = (const char*)d_in[9];
  A.out = (char*)d_out; A.ws = (char*)d_ws;
  fused_decode<<<256, 512, 0, stream>>>(A);
}
```

```cpp
#include <hip/hip_runtime.h>
#include <stdint.h>

struct Args { const void *x, *K, *V, *wq, *wo, *w1, *b1, *w2, *b2; void *out, *ws, *ws2; };

__shared__ __attribute__((aligned(16))) char smem[163840];

__global__ void __launch_bounds__(512, 2) fused_decode(Args A) {
#if defined(__HIP_DEVICE_COMPILE__)
  unsigned tid = threadIdx.x;
  const unsigned lds = (unsigned)(uintptr_t)(__attribute__((address_space(3))) char*)smem;
  asm volatile("" :: "s"(lds));
  asm volatile(
    "LSTART_%=:\n"
    "s_getpc_b64 s[12:13]\n"
    "s_load_dwordx16 s[16:31], s[0:1], 0\n"
    "s_load_dwordx8 s[32:39], s[0:1], 64\n"
    "v_lshrrev_b32 v16, 6, v0\n"
    "v_and_b32 v0, 63, v0\n"
    "v_readfirstlane_b32 s46, v16\n"
    "v_lshlrev_b32 v1, 4, v0\n"
    "s_and_b32 s47, s2, 7\n"
    "s_xor_b32 s47, s47, 0\n"
    "s_lshr_b32 s48, s2, 3\n"
    "s_mov_b32 s14, LEND_%=-LSTART_%=\n"
    "s_mov_b32 s15, 0x20000\n"
    "s_lshl_b32 s3, s48, 10\n"
    "s_cmp_eq_u32 s46, 5\n"
    "s_cbranch_scc0 SK1_%=\n"
    "buffer_load_dwordx4 v[64:67], v1, s[12:15], s3 offen\n"
    "SK1_%=:\n"
    "s_and_b32 s49, s48, 24\n"
    "s_add_u32 s49, s49, s47\n"
    "s_and_b32 s50, s48, 7\n"
    "s_lshl_b32 s51, s47, 5\n"
    "s_add_u32 s51, s51, s48\n"
    "s_sub_u32 s60, s48, 20\n"
    "s_lshr_b32 s61, s60, 1\n"
    "s_lshl_b32 s61, s61, 3\n"
    "s_add_u32 s61, s61, s47\n"
    "s_lshl_b32 s61, s61, 1\n"
    "s_and_b32 s3, s60, 1\n"
    "s_add_u32 s53, s61, s3\n"
    "s_cmp_lt_u32 s60, 8\n"
    "s_cselect_b32 s53, s53, 0xff\n"
    "s_cmp_lt_u32 s53, 54\n"
    "s_cselect_b32 s52, 2, 0\n"
    "s_cmp_lt_u32 s48, 20\n"
    "s_cselect_b32 s52, 1, s52\n"
    "v_and_b32 v13, 15, v0\n"
    "v_lshrrev_b32 v15, 4, v0\n"
    "v_lshrrev_b32 v16, 2, v0\n"
    "v_and_b32 v16, 4, v16\n"
    "v_xor_b32 v16, v16, v0\n"
    "v_lshlrev_b32 v24, 4, v16\n"
    "v_xor_b32 v25, 0x10, v24\n"
    "v_xor_b32 v26, 0x20, v24\n"
    "v_xor_b32 v27, 0x30, v24\n"
    "v_xor_b32 v28, 0x80, v24\n"
    "v_xor_b32 v29, 0x90, v24\n"
    "v_xor_b32 v30, 0xa0, v24\n"
    "v_xor_b32 v31, 0xb0, v24\n"
    "s_lshl_b32 s60, s49, 20\n"
    "s_lshl_b32 s55, s50, 10\n"
    "s_lshl_b32 s61, s46, 13\n"
    "s_add_u32 s60, s60, s61\n"
    "s_mul_i32 s65, s50, 655360\n"
    "s_lshl_b32 s3, s49, 8\n"
    "s_add_u32 s65, s65, s3\n"
    "s_mul_i32 s54, s50, 0\n"
    "s_mul_i32 s3, s46, 1\n"
    "s_add_u32 s54, s54, s3\n"
    "s_mul_i32 s3, s49, 1\n"
    "s_add_u32 s54, s54, s3\n"
    "s_and_b32 s54, s54, 15\n"
    "s_lshl_b32 s54, s54, 16\n"
    "s_add_u32 s54, s54, s55\n"
    "v_lshlrev_b32 v23, 13, v15\n"
    "v_lshl_add_u32 v23, v13, 4, v23\n"
    "s_mul_i32 s3, s50, 160\n"
    "v_lshlrev_b32 v22, 2, v15\n"
    "v_lshl_add_u32 v22, v13, 4, v22\n"
    "v_add_u32 v21, s3, v22\n"
    "v_mov_b32 v20, 0x40000000\n"
    "v_cmp_gt_u32 vcc, 10, v13\n"
    "s_nop 1\n"
    "v_cndmask_b32 v21, v20, v21, vcc\n"
    "v_lshlrev_b32 v5, 2, v0\n"
    "s_waitcnt lgkmcnt(0)\n"
    "s_load_dwordx2 s[44:45], s[36:37], 0\n"
    "s_mov_b32 s40, s36\n"
    "s_mov_b32 s41, s37\n"
    "s_mov_b32 s42, 0x30c000\n"
    "s_mov_b32 s43, 0x20000\n"
    "s_memrealtime s[66:67]\n"
    "s_cmp_eq_u32 s46, 4\n"
    "s_cbranch_scc0 SK2_%=\n"
    "s_mov_b32 s12, s16\n"
    "s_mov_b32 s13, s17\n"
    "s_mov_b32 s14, 1280\n"
    "s_mov_b32 s15, 0x20000\n"
    "buffer_load_dwordx4 v[32:35], v1, s[12:15], 0 offen\n"
    "buffer_load_dword v36, v5, s[12:15], 0 offen offset:1024\n"
    "buffer_load_dword v37, v21, s[12:15], 0 offen\n"
    "SK2_%=:\n"
    "s_cmp_eq_u32 s46, 6\n"
    "s_cbranch_scc0 SK3_%=\n"
    "s_mov_b32 s12, s32\n"
    "s_mov_b32 s13, s33\n"
    "s_mov_b32 s14, 1280\n"
    "s_mov_b32 s15, 0x20000\n"
    "s_lshl_b32 s3, s53, 7\n"
    "v_cmp_gt_u32 vcc, 8, v13\n"
    "s_nop 1\n"
    "v_cndmask_b32 v19, v20, v22, vcc\n"
    "v_add_u32 v19, s3, v19\n"
    "buffer_load_dword v32, v21, s[12:15], 0 offen\n"
    "buffer_load_dword v33, v19, s[12:15], 0 offen\n"
    "SK3_%=:\n"
    "s_cmp_eq_u32 s46, 7\n"
    "s_cbranch_scc0 SK4_%=\n"
    "v_mov_b32 v32, -1\n"
    "v_mov_b32 v33, -1\n"
    "v_mov_b32 v34, -1\n"
    "v_mov_b32 v35, -1\n"
    "s_mul_i32 s3, s51, 1408\n"
    "s_add_u32 s3, s3, 491520\n"
    "v_cmp_gt_u32 vcc, 24, v0\n"
    "s_nop 1\n"
    "v_cndmask_b32 v19, v20, v1, vcc\n"
    "buffer_store_dwordx4 v[32:35], v1, s[40:43], s3 offen sc1\n"
    "buffer_store_dwordx4 v[32:35], v19, s[40:43], s3 offen offset:1024 sc1\n"
    "s_mul_i32 s3, s51, 2240\n"
    "s_add_u32 s3, s3, 2621440\n"
    "v_cmp_gt_u32 vcc, 12, v0\n"
    "s_nop 1\n"
    "v_cndmask_b32 v19, v20, v1, vcc\n"
    "buffer_store_dwordx4 v[32:35], v1, s[40:43], s3 offen sc1\n"
    "buffer_store_dwordx4 v[32:35], v1, s[40:43], s3 offen offset:1024 sc1\n"
    "buffer_store_dwordx4 v[32:35], v19, s[40:43], s3 offen offset:2048 sc1\n"
    "s_cmp_eq_u32 s52, 1\n"
    "s_cbranch_scc0 SK5_%=\n"
    "s_cmp_ge_u32 s48, 10\n"
    "s_cselect_b32 s3, 1, 0\n"
    "s_mul_i32 s61, s3, 10\n"
    "s_sub_u32 s61, s48, s61\n"
    "s_lshl_b32 s61, s61, 7\n"
    "s_lshl_b32 s3, s3, 3\n"
    "s_add_u32 s3, s3, s47\n"
    "s_mul_i32 s3, s3, 1280\n"
    "s_add_u32 s3, s3, s61\n"
    "v_lshrrev_b32 v19, 3, v0\n"
    "v_mul_u32_u24 v19, 20480, v19\n"
    "v_and_b32 v18, 7, v0\n"
    "v_lshl_add_u32 v19, v18, 4, v19\n"
    "s_add_u32 s3, s3, 163840\n"
    "s_add_u32 s61, s3, 163840\n"
    "buffer_store_dwordx4 v[32:35], v19, s[40:43], s3 offen sc1\n"
    "buffer_store_dwordx4 v[32:35], v19, s[40:43], s61 offen sc1\n"
    "SK5_%=:\n"
    "s_cmp_lt_u32 s53, 56\n"
    "s_cbranch_scc0 SK6_%=\n"
    "s_mul_i32 s3, s53, 1280\n"
    "s_add_u32 s3, s3, 16384\n"
    "s_add_u32 s61, s3, 71680\n"
    "v_cmp_gt_u32 vcc, 16, v0\n"
    "s_nop 1\n"
    "v_cndmask_b32 v19, v20, v1, vcc\n"
    "buffer_store_dwordx4 v[32:35], v1, s[40:43], s3 offen sc1\n"
    "buffer_store_dwordx4 v[32:35], v19, s[40:43], s3 offen offset:1024 sc1\n"
    "buffer_store_dwordx4 v[32:35], v1, s[40:43], s61 offen sc1\n"
    "buffer_store_dwordx4 v[32:35], v19, s[40:43], s61 offen offset:1024 sc1\n"
    "SK6_%=:\n"
    "SK4_%=:\n"
    "s_add_u32 s12, s22, s65\n"
    "s_addc_u32 s13, s23, 0\n"
    "s_mov_b32 s14, 647424\n"
    "s_mov_b32 s15, 0x20000\n"
    "s_lshl_b32 s3, s46, 10\n"
    "s_add_u32 m0, s3, 131072\n"
    "s_lshl_b32 s61, s46, 15\n"
    "s_add_u32 s4, s18, s60\n"
    "s_addc_u32 s5, s19, 0\n"
    "s_mov_b32 s6, 991232\n"
    "s_mov_b32 s7, 0x20000\n"
    "buffer_load_dwordx4 v23, s[12:15], s61 offen nt lds\n"
    "s_add_u32 m0, m0, 8192\n"
    "s_add_u32 s61, s61, 262144\n"
    "buffer_load_dwordx4 v23, s[12:15], s61 offen nt lds\n"
    "s_cmp_lt_u32 s46, 4\n"
    "s_cbranch_scc0 SK7_%=\n"
    "s_add_u32 m0, m0, 8192\n"
    "s_add_u32 s61, s61, 262144\n"
    "buffer_load_dwordx4 v23, s[12:15], s61 offen nt lds\n"
    "SK7_%=:\n"
    "s_lshl_b32 m0, s46, 14\n"
    "s_add_u32 s8, s20, s60\n"
    "s_addc_u32 s9, s21, 0\n"
    "s_mov_b32 s10, 991232\n"
    "s_mov_b32 s11, 0x20000\n"
    "s_mov_b32 s55, s54\n"
    "buffer_load_dwordx4 v24, s[4:7], s54 offen nt lds\n"
    "s_add_u32 m0, m0, 1024\n"
    "s_add_u32 s54, s54, 68608\n"
    "s_and_b32 s54, s54, 990208\n"
    "buffer_load_dwordx4 v25, s[4:7], s54 offen nt lds\n"
    "s_add_u32 m0, m0, 1024\n"
    "s_add_u32 s54, s54, 68608\n"
    "s_and_b32 s54, s54, 990208\n"
    "buffer_load_dwordx4 v26, s[4:7], s54 offen nt lds\n"
    "s_add_u32 m0, m0, 1024\n"
    "s_add_u32 s54, s54, 68608\n"
    "s_and_b32 s54, s54, 990208\n"
    "buffer_load_dwordx4 v27, s[4:7], s54 offen nt lds\n"
    "s_add_u32 m0, m0, 1024\n"
    "s_add_u32 s54, s54, 68608\n"
    "s_and_b32 s54, s54, 990208\n"
    "buffer_load_dwordx4 v24, s[4:7], s54 offen nt lds\n"
    "s_add_u32 m0, m0, 1024\n"
    "s_add_u32 s54, s54, 68608\n"
    "s_and_b32 s54, s54, 990208\n"
    "buffer_load_dwordx4 v25, s[4:7], s54 offen nt lds\n"
    "s_add_u32 m0, m0, 1024\n"
    "s_add_u32 s54, s54, 68608\n"
    "s_and_b32 s54, s54, 990208\n"
    "buffer_load_dwordx4 v26, s[4:7], s54 offen nt lds\n"
    "s_add_u32 m0, m0, 1024\n"
    "s_add_u32 s54, s54, 68608\n"
    "s_and_b32 s54, s54, 990208\n"
    "buffer_load_dwordx4 v27, s[4:7], s54 offen nt lds\n"
    "s_add_u32 m0, m0, 1024\n"
    "s_add_u32 s54, s54, 68608\n"
    "s_and_b32 s54, s54, 990208\n"
    "v_and_b32 v16, 1, v0\n"
    "v_mul_u32_u24 v2, 0x5555, v16\n"
    "v_add_u32 v2, 0x8888, v2\n"
    "v_mul_u32_u24 v3, 0xaaaa, v16\n"
    "v_add_u32 v3, 0x4444, v3\n"
    "v_lshrrev_b32 v17, 1, v0\n"
    "v_and_b32 v17, 2, v17\n"
    "v_or_b32 v17, v17, v16\n"
    "v_mul_u32_u24 v4, 0x5555, v17\n"
    "v_add_u32 v5, 151552, v5\n"
    "v_and_b32 v17, 10, v0\n"
    "v_mov_b32 v18, 0x3f80\n"
    "v_cmp_eq_u32 vcc, 0, v17\n"
    "s_nop 1\n"
    "v_cndmask_b32 v6, 0, v18, vcc\n"
    "v_cndmask_b32 v8, 0, v18, vcc\n"
    "v_cmp_eq_u32 vcc, 8, v17\n"
    "s_nop 1\n"
    "v_cndmask_b32 v7, 0, v18, vcc\n"
    "v_cndmask_b32 v9, 0, v18, vcc\n"
    "v_and_b32 v17, 11, v0\n"
    "v_lshlrev_b32 v12, 10, v17\n"
    "v_and_b32 v17, 4, v0\n"
    "v_lshl_add_u32 v12, v17, 6, v12\n"
    "v_xor_b32 v17, v15, v13\n"
    "v_lshl_add_u32 v12, v17, 4, v12\n"
    "s_lshl_b32 s3, s46, 14\n"
    "v_add_u32 v12, s3, v12\n"
    "v_lshlrev_b32 v14, 5, v15\n"
    "v_lshl_add_u32 v14, v16, 4, v14\n"
    "v_add_u32 v14, 152320, v14\n"
    "s_mov_b32 s56, 0\n"
    "s_mov_b32 s57, 0x80000000\n"
    "s_mov_b32 s58, 0x00010000\n"
    "s_mov_b32 s59, 0x00010000\n"
    "s_mov_b32 s32, 0x3acccccd\n"
    "s_mov_b32 s33, 0x3f800054\n"
    "s_mov_b32 s62, 0x3db504f3\n"
    "s_mov_b32 s63, 0x3fb8aa3b\n"
    "s_mov_b32 s64, 262144\n"
    "s_mov_b32 s38, 0x05040100\n"
    "s_mov_b32 s39, 0x07060302\n"
    "s_cmp_eq_u32 s46, 4\n"
    "s_cbranch_scc0 SK8_%=\n"
    "s_waitcnt vmcnt(10)\n"
    "v_mov_b32 v18, 0x3f803f80\n"
    "v_mov_b32 v16, 0\n"
    "v_mov_b32 v17, 0\n"
    "v_dot2_f32_bf16 v16, v32, v18, v16\n"
    "v_dot2_f32_bf16 v17, v32, v32, v17\n"
    "v_dot2_f32_bf16 v16, v33, v18, v16\n"
    "v_dot2_f32_bf16 v17, v33, v33, v17\n"
    "v_dot2_f32_bf16 v16, v34, v18, v16\n"
    "v_dot2_f32_bf16 v17, v34, v34, v17\n"
    "v_dot2_f32_bf16 v16, v35, v18, v16\n"
    "v_dot2_f32_bf16 v17, v35, v35, v17\n"
    "v_dot2_f32_bf16 v16, v36, v18, v16\n"
    "v_dot2_f32_bf16 v17, v36, v36, v17\n"
    "s_nop 1\n"
    "v_add_f32_dpp v16, v16, v16 row_ror:8 row_mask:0xf bank_mask:0xf\n"
    "v_add_f32_dpp v17, v17, v17 row_ror:8 row_mask:0xf bank_mask:0xf\n"
    "s_nop 0\n"
    "v_add_f32_dpp v16, v16, v16 row_ror:4 row_mask:0xf bank_mask:0xf\n"
    "v_add_f32_dpp v17, v17, v17 row_ror:4 row_mask:0xf bank_mask:0xf\n"
    "s_nop 0\n"
    "v_add_f32_dpp v16, v16, v16 row_ror:2 row_mask:0xf bank_mask:0xf\n"
    "v_add_f32_dpp v17, v17, v17 row_ror:2 row_mask:0xf bank_mask:0xf\n"
    "s_nop 0\n"
    "v_add_f32_dpp v16, v16, v16 row_ror:1 row_mask:0xf bank_mask:0xf\n"
    "v_add_f32_dpp v17, v17, v17 row_ror:1 row_mask:0xf bank_mask:0xf\n"
    "v_mov_b32 v18, v16\n"
    "s_nop 1\n"
    "v_permlane32_swap_b32 v18, v16\n"
    "v_add_f32 v16, v18, v16\n"
    "v_mov_b32 v18, v16\n"
    "s_nop 1\n"
    "v_permlane16_swap_b32 v18, v16\n"
    "v_add_f32 v16, v18, v16\n"
    "v_mov_b32 v18, v17\n"
    "s_nop 1\n"
    "v_permlane32_swap_b32 v18, v17\n"
    "v_add_f32 v17, v18, v17\n"
    "v_mov_b32 v18, v17\n"
    "s_nop 1\n"
    "v_permlane16_swap_b32 v18, v17\n"
    "v_add_f32 v17, v18, v17\n"
    "v_mul_f32 v16, 0x3acccccd, v16\n"
    "v_mul_f32 v17, 0x3acccccd, v17\n"
    "v_fma_f32 v17, -v16, v16, v17\n"
    "v_max_f32 v17, 0, v17\n"
    "v_cvt_pk_bf16_f32 v16, v16, v17\n"
    "v_and_b32 v17, 0xffff0000, v16\n"
    "v_lshlrev_b32 v16, 16, v16\n"
    "v_add_f32 v17, 0x3727c5ac, v17\n"
    "v_cvt_pk_bf16_f32 v17, v17, v17\n"
    "v_lshlrev_b32 v17, 16, v17\n"
    "v_sqrt_f32 v17, v17\n"
    "s_nop 0\n"
    "v_cvt_pk_bf16_f32 v17, v17, v17\n"
    "v_lshlrev_b32 v17, 16, v17\n"
    "v_rcp_f32 v17, v17\n"
    "v_lshlrev_b32 v18, 16, v37\n"
    "v_and_b32 v19, 0xffff0000, v37\n"
    "v_sub_f32 v18, v18, v16\n"
    "v_sub_f32 v19, v19, v16\n"
    "v_cvt_pk_bf16_f32 v18, v18, v19\n"
    "v_and_b32 v19, 0xffff0000, v18\n"
    "v_lshlrev_b32 v18, 16, v18\n"
    "v_mul_f32 v18, v18, v17\n"
    "v_mul_f32 v19, v19, v17\n"
    "v_cvt_pk_bf16_f32 v18, v18, v19\n"
    "ds_write_b32 v5, v18 offset:0\n"
    "SK8_%=:\n"
    "s_cmp_eq_u32 s46, 6\n"
    "s_cbranch_scc0 SK9_%=\n"
    "v_cmp_gt_u32 vcc, 32, v0\n"
    "s_nop 1\n"
    "v_cndmask_b32 v17, 0, -1, vcc\n"
    "ds_write_b32 v5, v17 offset:1024\n"
    "v_mov_b32 v17, -1\n"
    "ds_write_b32 v5, v17 offset:768\n"
    "s_waitcnt vmcnt(10)\n"
    "ds_write_b32 v5, v32 offset:256\n"
    "ds_write_b32 v5, v33 offset:512\n"
    "SK9_%=:\n"
    "s_waitcnt vmcnt(8)\n"
    "s_waitcnt lgkmcnt(0)\n"
    "s_barrier\n"
    "s_add_u32 s44, s44, 0x7f4a7c15\n"
    "s_addc_u32 s45, s45, 0x9e3779b9\n"
    "s_lshr_b32 s3, s52, 1\n"
    "s_mov_b32 s3, 0\n"
    "s_add_u32 s3, s3, 4\n"
    "s_cmp_lt_u32 s46, s3\n"
    "s_mov_b32 s37, 43\n"
    "s_mov_b32 s36, 388\n"
    "s_cselect_b32 s37, 24, s37\n"
    "s_cselect_b32 s36, 292, s36\n"
    "s_add_u32 s36, s36, s66\n"
    "s_cmp_eq_u32 s46, 7\n"
    "s_cbranch_scc0 SK10_%=\n"
    "s_waitcnt vmcnt(8)\n"
    "v_mov_b32 v16, s44\n"
    "v_mov_b32 v17, s45\n"
    "v_cmp_eq_u32 vcc, 0, v0\n"
    "s_nop 1\n"
    "v_cndmask_b32 v19, v20, v0, vcc\n"
    "s_lshl_b32 s3, s51, 3\n"
    "s_add_u32 s3, s3, 4096\n"
    "buffer_store_dwordx2 v[16:17], v19, s[40:43], s3 offen sc1\n"
    "SK10_%=:\n"
    "s_cmp_lt_u32 s46, 4\n"
    "s_cbranch_scc0 SK11_%=\n"
    "v_lshrrev_b32 v17, 1, v15\n"
    "v_lshlrev_b32 v17, 9, v17\n"
    "v_lshl_add_u32 v17, v13, 2, v17\n"
    "s_lshl_b32 s3, s46, 6\n"
    "s_add_u32 s3, s3, 131072\n"
    "v_add_u32 v17, s3, v17\n"
    "v_and_b32 v16, 1, v15\n"
    "v_lshl_add_u32 v16, v16, 12, v17\n"
    "ds_read_b32 v18, v5 offset:0\n"
    "ds_read2st64_b32 v[40:41], v16 offset0:0 offset1:1\n"
    "ds_read2st64_b32 v[42:43], v16 offset0:8 offset1:9\n"
    "ds_read2st64_b32 v[44:45], v16 offset0:4 offset1:5\n"
    "ds_read2st64_b32 v[46:47], v16 offset0:12 offset1:13\n"
    "ds_read2st64_b32 v[48:49], v16 offset0:32 offset1:33\n"
    "ds_read2st64_b32 v[50:51], v16 offset0:40 offset1:41\n"
    "ds_read2st64_b32 v[52:53], v16 offset0:36 offset1:37\n"
    "ds_read2st64_b32 v[54:55], v16 offset0:44 offset1:45\n"
    "ds_read2st64_b32 v[56:57], v17 offset0:64 offset1:65\n"
    "ds_read2st64_b32 v[58:59], v17 offset0:72 offset1:73\n"
    "ds_read2st64_b32 v[60:61], v17 offset0:68 offset1:69\n"
    "ds_read2st64_b32 v[62:63], v17 offset0:76 offset1:77\n"
    "v_mov_b32 v224, 0x3f803f80\n"
    "v_mov_b32 v225, 0x3f803f80\n"
    "v_mov_b32 v226, 0x3f803f80\n"
    "v_mov_b32 v227, 0x3f803f80\n"
    "v_mov_b32 v228, 0x3f803f80\n"
    "v_mov_b32 v229, 0x3f803f80\n"
    "v_mov_b32 v230, 0x3f803f80\n"
    "v_mov_b32 v231, 0x3f803f80\n"
    "v_mov_b32 v232, 0x3f803f80\n"
    "v_mov_b32 v233, 0x3f803f80\n"
    "v_mov_b32 v234, 0\n"
    "v_mov_b32 v235, 0\n"
    "v_mov_b32 v236, 0\n"
    "v_mov_b32 v237, 0\n"
    "v_mov_b32 v238, 0\n"
    "v_mov_b32 v239, 0\n"
    "s_lshl_b32 s3, s46, 8\n"
    "v_lshl_add_u32 v19, v13, 4, s3\n"
    "v_and_b32 v16, 1, v15\n"
    "v_lshl_add_u32 v19, v16, 10, v19\n"
    "v_add_u32 v16, 1, v15\n"
    "v_and_b32 v16, 2, v16\n"
    "v_lshl_add_u32 v19, v16, 29, v19\n"
    "s_lshl_b32 s3, s51, 11\n"
    "s_add_u32 s3, s3, 2097152\n"
    "v_mov_b32 v33, s44\n"
    "v_mov_b32 v35, s44\n"
    "s_waitcnt lgkmcnt(0)\n"
    "v_mov_b32_dpp v224, v18 row_newbcast:0 row_mask:0xf bank_mask:0x7\n"
    "v_mov_b32_dpp v225, v18 row_newbcast:1 row_mask:0xf bank_mask:0x7\n"
    "v_mov_b32_dpp v226, v18 row_newbcast:2 row_mask:0xf bank_mask:0x7\n"
    "v_mov_b32_dpp v227, v18 row_newbcast:3 row_mask:0xf bank_mask:0x7\n"
    "v_mov_b32_dpp v228, v18 row_newbcast:4 row_mask:0xf bank_mask:0x7\n"
    "v_mov_b32_dpp v229, v18 row_newbcast:5 row_mask:0xf bank_mask:0x7\n"
    "v_mov_b32_dpp v230, v18 row_newbcast:6 row_mask:0xf bank_mask:0x7\n"
    "v_mov_b32_dpp v231, v18 row_newbcast:7 row_mask:0xf bank_mask:0x7\n"
    "v_mov_b32_dpp v232, v18 row_newbcast:8 row_mask:0xf bank_mask:0x7\n"
    "v_mov_b32_dpp v233, v18 row_newbcast:9 row_mask:0xf bank_mask:0x7\n"
    "v_smfmac_f32_16x16x64_bf16 v[236:239], v[224:227], v[40:47], v2\n"
    "v_smfmac_f32_16x16x64_bf16 v[236:239], v[228:231], v[48:55], v2\n"
    "v_smfmac_f32_16x16x64_bf16 v[236:239], v[232:235], v[56:63], v2\n"
    "s_nop 7\n"
    "v_mov_b32 v32, v236\n"
    "v_mov_b32 v34, v237\n"
    "buffer_store_dwordx4 v[32:35], v19, s[40:43], s3 offen sc1\n"
    "SK11_%=:\n"
    "buffer_load_dwordx4 v28, s[4:7], s54 offen nt lds\n"
    "s_add_u32 m0, m0, 1024\n"
    "s_add_u32 s54, s54, 68608\n"
    "s_and_b32 s54, s54, 990208\n"
    "PC12_%=:\n"
    "s_memrealtime s[12:13]\n"
    "s_waitcnt lgkmcnt(0)\n"
    "s_sub_u32 s12, s12, s36\n"
    "s_cmp_lt_i32 s12, 0\n"
    "s_cbranch_scc1 PC12_%=\n"
    "s_add_u32 s36, s36, s37\n"
    "buffer_load_dwordx4 v29, s[4:7], s54 offen nt lds\n"
    "s_add_u32 m0, m0, 1024\n"
    "s_add_u32 s54, s54, 68608\n"
    "s_and_b32 s54, s54, 990208\n"
    "PC13_%=:\n"
    "s_memrealtime s[12:13]\n"
    "s_waitcnt lgkmcnt(0)\n"
    "s_sub_u32 s12, s12, s36\n"
    "s_cmp_lt_i32 s12, 0\n"
    "s_cbranch_scc1 PC13_%=\n"
    "s_add_u32 s36, s36, s37\n"
    "buffer_load_dwordx4 v30, s[4:7], s54 offen nt lds\n"
    "s_add_u32 m0, m0, 1024\n"
    "s_add_u32 s54, s54, 68608\n"
    "s_and_b32 s54, s54, 990208\n"
    "PC14_%=:\n"
    "s_memrealtime s[12:13]\n"
    "s_waitcnt lgkmcnt(0)\n"
    "s_sub_u32 s12, s12, s36\n"
    "s_cmp_lt_i32 s12, 0\n"
    "s_cbranch_scc1 PC14_%=\n"
    "s_add_u32 s36, s36, s37\n"
    "buffer_load_dwordx4 v31, s[4:7], s54 offen nt lds\n"
    "s_add_u32 m0, m0, 1024\n"
    "s_add_u32 s54, s54, 68608\n"
    "s_and_b32 s54, s54, 990208\n"
    "PC15_%=:\n"
    "s_memrealtime s[12:13]\n"
    "s_waitcnt lgkmcnt(0)\n"
    "s_sub_u32 s12, s12, s36\n"
    "s_cmp_lt_i32 s12, 0\n"
    "s_cbranch_scc1 PC15_%=\n"
    "s_add_u32 s36, s36, s37\n"
    "buffer_load_dwordx4 v28, s[4:7], s54 offen nt lds\n"
    "s_add_u32 m0, m0, 1024\n"
    "s_add_u32 s54, s54, 68608\n"
    "s_and_b32 s54, s54, 990208\n"
    "PC16_%=:\n"
    "s_memrealtime s[12:13]\n"
    "s_waitcnt lgkmcnt(0)\n"
    "s_sub_u32 s12, s12, s36\n"
    "s_cmp_lt_i32 s12, 0\n"
    "s_cbranch_scc1 PC16_%=\n"
    "s_add_u32 s36, s36, s37\n"
    "buffer_load_dwordx4 v29, s[4:7], s54 offen nt lds\n"
    "s_add_u32 m0, m0, 1024\n"
    "s_add_u32 s54, s54, 68608\n"
    "s_and_b32 s54, s54, 990208\n"
    "PC17_%=:\n"
    "s_memrealtime s[12:13]\n"
    "s_waitcnt lgkmcnt(0)\n"
    "s_sub_u32 s12, s12, s36\n"
    "s_cmp_lt_i32 s12, 0\n"
    "s_cbranch_scc1 PC17_%=\n"
    "s_add_u32 s36, s36, s37\n"
    "buffer_load_dwordx4 v30, s[4:7], s54 offen nt lds\n"
    "s_add_u32 m0, m0, 1024\n"
    "s_add_u32 s54, s54, 68608\n"
    "s_and_b32 s54, s54, 990208\n"
    "PC18_%=:\n"
    "s_memrealtime s[12:13]\n"
    "s_waitcnt lgkmcnt(0)\n"
    "s_sub_u32 s12, s12, s36\n"
    "s_cmp_lt_i32 s12, 0\n"
    "s_cbranch_scc1 PC18_%=\n"
    "s_add_u32 s36, s36, s37\n"
    "buffer_load_dwordx4 v31, s[4:7], s54 offen nt lds\n"
    "s_add_u32 m0, m0, 1024\n"
    "s_add_u32 s54, s54, 68608\n"
    "s_and_b32 s54, s54, 990208\n"
    "s_lshl_b32 s3, s46, 8\n"
    "v_lshl_add_u32 v19, v13, 4, s3\n"
    "v_lshl_add_u32 v19, v15, 11, v19\n"
    "s_and_b32 s60, s51, 0xf8\n"
    "s_lshl_b32 s60, s60, 11\n"
    "s_add_u32 s60, s60, 2097152\n"
    "s_add_u32 s61, s60, 8192\n"
    "s_cmp_eq_u32 s46, 4\n"
    "s_cselect_b32 s60, 4096, s60\n"
    "s_lshl_b32 s3, s46, 2\n"
    "s_add_u32 s3, s3, 152672\n"
    "v_mov_b32 v18, s3\n"
    "PC19_%=:\n"
    "s_memrealtime s[12:13]\n"
    "s_waitcnt lgkmcnt(0)\n"
    "s_sub_u32 s12, s12, s36\n"
    "s_cmp_lt_i32 s12, 0\n"
    "s_cbranch_scc1 PC19_%=\n"
    "s_add_u32 s36, s36, s37\n"
    "buffer_load_dwordx4 v[64:67], v1, s[8:11], s55 offen nt\n"
    "s_add_u32 s55, s55, 68608\n"
    "s_and_b32 s55, s55, 990208\n"
    "PC20_%=:\n"
    "s_memrealtime s[12:13]\n"
    "s_waitcnt lgkmcnt(0)\n"
    "s_sub_u32 s12, s12, s36\n"
    "s_cmp_lt_i32 s12, 0\n"
    "s_cbranch_scc1 PC20_%=\n"
    "s_add_u32 s36, s36, s37\n"
    "buffer_load_dwordx4 v[68:71], v1, s[8:11], s55 offen nt\n"
    "s_add_u32 s55, s55, 68608\n"
    "s_and_b32 s55, s55, 990208\n"
    "PC21_%=:\n"
    "s_memrealtime s[12:13]\n"
    "s_waitcnt lgkmcnt(0)\n"
    "s_sub_u32 s12, s12, s36\n"
    "s_cmp_lt_i32 s12, 0\n"
    "s_cbranch_scc1 PC21_%=\n"
    "s_add_u32 s36, s36, s37\n"
    "buffer_load_dwordx4 v[72:75], v1, s[8:11], s55 offen nt\n"
    "s_add_u32 s55, s55, 68608\n"
    "s_and_b32 s55, s55, 990208\n"
    "s_cmp_eq_u32 s37, 43\n"
    "s_cselect_b32 s37, 16, s37\n"
    "PC22_%=:\n"
    "s_memrealtime s[12:13]\n"
    "s_waitcnt lgkmcnt(0)\n"
    "s_sub_u32 s12, s12, s36\n"
    "s_cmp_lt_i32 s12, 0\n"
    "s_cbranch_scc1 PC22_%=\n"
    "s_add_u32 s36, s36, s37\n"
    "buffer_load_dwordx4 v[76:79], v1, s[8:11], s55 offen nt\n"
    "s_add_u32 s55, s55, 68608\n"
    "s_and_b32 s55, s55, 990208\n"
    "PC23_%=:\n"
    "s_memrealtime s[12:13]\n"
    "s_waitcnt lgkmcnt(0)\n"
    "s_sub_u32 s12, s12, s36\n"
    "s_cmp_lt_i32 s12, 0\n"
    "s_cbranch_scc1 PC23_%=\n"
    "s_add_u32 s36, s36, s37\n"
    "buffer_load_dwordx4 v[80:83], v1, s[8:11], s55 offen nt\n"
    "s_add_u32 s55, s55, 68608\n"
    "s_and_b32 s55, s55, 990208\n"
    "PC24_%=:\n"
    "s_memrealtime s[12:13]\n"
    "s_waitcnt lgkmcnt(0)\n"
    "s_sub_u32 s12, s12, s36\n"
    "s_cmp_lt_i32 s12, 0\n"
    "s_cbranch_scc1 PC24_%=\n"
    "s_add_u32 s36, s36, s37\n"
    "buffer_load_dwordx4 v[84:87], v1, s[8:11], s55 offen nt\n"
    "s_add_u32 s55, s55, 68608\n"
    "s_and_b32 s55, s55, 990208\n"
    "PC25_%=:\n"
    "s_memrealtime s[12:13]\n"
    "s_waitcnt lgkmcnt(0)\n"
    "s_sub_u32 s12, s12, s36\n"
    "s_cmp_lt_i32 s12, 0\n"
    "s_cbranch_scc1 PC25_%=\n"
    "s_add_u32 s36, s36, s37\n"
    "buffer_load_dwordx4 v[88:91], v1, s[8:11], s55 offen nt\n"
    "s_add_u32 s55, s55, 68608\n"
    "s_and_b32 s55, s55, 990208\n"
    "s_cmp_lt_u32 s46, 4\n"
    "s_cbranch_scc0 SK26_%=\n"
    "buffer_load_dwordx4 v[192:195], v19, s[40:43], s60 offen sc1\n"
    "buffer_load_dwordx4 v[200:203], v19, s[40:43], s60 offen offset:1024 sc1\n"
    "buffer_load_dwordx4 v[196:199], v19, s[40:43], s61 offen sc1\n"
    "buffer_load_dwordx4 v[204:207], v19, s[40:43], s61 offen offset:1024 sc1\n"
    "SK26_%=:\n"
    "s_cmp_eq_u32 s46, 4\n"
    "s_cbranch_scc0 SK27_%=\n"
    "buffer_load_dwordx4 v[192:195], v1, s[40:43], s60 offen sc1\n"
    "buffer_load_dwordx4 v[196:199], v1, s[40:43], s60 offen offset:1024 sc1\n"
    "SK27_%=:\n"
    "PC28_%=:\n"
    "s_memrealtime s[12:13]\n"
    "s_waitcnt lgkmcnt(0)\n"
    "s_sub_u32 s12, s12, s36\n"
    "s_cmp_lt_i32 s12, 0\n"
    "s_cbranch_scc1 PC28_%=\n"
    "s_add_u32 s36, s36, s37\n"
    "buffer_load_dwordx4 v[92:95], v1, s[8:11], s55 offen nt\n"
    "s_add_u32 s55, s55, 68608\n"
    "s_and_b32 s55, s55, 990208\n"
    "PC29_%=:\n"
    "s_memrealtime s[12:13]\n"
    "s_waitcnt lgkmcnt(0)\n"
    "s_sub_u32 s12, s12, s36\n"
    "s_cmp_lt_i32 s12, 0\n"
    "s_cbranch_scc1 PC29_%=\n"
    "s_add_u32 s36, s36, s37\n"
    "buffer_load_dwordx4 v[96:99], v1, s[8:11], s55 offen nt\n"
    "s_add_u32 s55, s55, 68608\n"
    "s_and_b32 s55, s55, 990208\n"
    "PC30_%=:\n"
    "s_memrealtime s[12:13]\n"
    "s_waitcnt lgkmcnt(0)\n"
    "s_sub_u32 s12, s12, s36\n"
    "s_cmp_lt_i32 s12, 0\n"
    "s_cbranch_scc1 PC30_%=\n"
    "s_add_u32 s36, s36, s37\n"
    "buffer_load_dwordx4 v[100:103], v1, s[8:11], s55 offen nt\n"
    "s_add_u32 s55, s55, 68608\n"
    "s_and_b32 s55, s55, 990208\n"
    "PC31_%=:\n"
    "s_memrealtime s[12:13]\n"
    "s_waitcnt lgkmcnt(0)\n"
    "s_sub_u32 s12, s12, s36\n"
    "s_cmp_lt_i32 s12, 0\n"
    "s_cbranch_scc1 PC31_%=\n"
    "s_add_u32 s36, s36, s37\n"
    "buffer_load_dwordx4 v[104:107], v1, s[8:11], s55 offen nt\n"
    "s_add_u32 s55, s55, 68608\n"
    "s_and_b32 s55, s55, 990208\n"
    "PC32_%=:\n"
    "s_memrealtime s[12:13]\n"
    "s_waitcnt lgkmcnt(0)\n"
    "s_sub_u32 s12, s12, s36\n"
    "s_cmp_lt_i32 s12, 0\n"
    "s_cbranch_scc1 PC32_%=\n"
    "s_add_u32 s36, s36, s37\n"
    "buffer_load_dwordx4 v[108:111], v1, s[8:11], s55 offen nt\n"
    "s_add_u32 s55, s55, 68608\n"
    "s_and_b32 s55, s55, 990208\n"
    "PC33_%=:\n"
    "s_memrealtime s[12:13]\n"
    "s_waitcnt lgkmcnt(0)\n"
    "s_sub_u32 s12, s12, s36\n"
    "s_cmp_lt_i32 s12, 0\n"
    "s_cbranch_scc1 PC33_%=\n"
    "s_add_u32 s36, s36, s37\n"
    "buffer_load_dwordx4 v[112:115], v1, s[8:11], s55 offen nt\n"
    "s_add_u32 s55, s55, 68608\n"
    "s_and_b32 s55, s55, 990208\n"
    "PC34_%=:\n"
    "s_memrealtime s[12:13]\n"
    "s_waitcnt lgkmcnt(0)\n"
    "s_sub_u32 s12, s12, s36\n"
    "s_cmp_lt_i32 s12, 0\n"
    "s_cbranch_scc1 PC34_%=\n"
    "s_add_u32 s36, s36, s37\n"
    "buffer_load_dwordx4 v[116:119], v1, s[8:11], s55 offen nt\n"
    "s_add_u32 s55, s55, 68608\n"
    "s_and_b32 s55, s55, 990208\n"
    "PC35_%=:\n"
    "s_memrealtime s[12:13]\n"
    "s_waitcnt lgkmcnt(0)\n"
    "s_sub_u32 s12, s12, s36\n"
    "s_cmp_lt_i32 s12, 0\n"
    "s_cbranch_scc1 PC35_%=\n"
    "s_add_u32 s36, s36, s37\n"
    "buffer_load_dwordx4 v[120:123], v1, s[8:11], s55 offen nt\n"
    "s_add_u32 s55, s55, 68608\n"
    "s_and_b32 s55, s55, 990208\n"
    "PC36_%=:\n"
    "s_memrealtime s[12:13]\n"
    "s_waitcnt lgkmcnt(0)\n"
    "s_sub_u32 s12, s12, s36\n"
    "s_cmp_lt_i32 s12, 0\n"
    "s_cbranch_scc1 PC36_%=\n"
    "s_add_u32 s36, s36, s37\n"
    "buffer_load_dwordx4 v[124:127], v1, s[8:11], s55 offen nt\n"
    "s_add_u32 s55, s55, 68608\n"
    "s_and_b32 s55, s55, 990208\n"
    "ds_write_b32 v18, v18\n"
    "s_waitcnt vmcnt(16)\n"
    "v_xor_b32 v21, 64, v12\n"
    "v_xor_b32 v22, 0x80, v12\n"
    "v_xor_b32 v23, 0xc0, v12\n"
    "ds_read_b128 v[128:131], v12 offset:0\n"
    "ds_read_b128 v[132:135], v21 offset:0\n"
    "ds_read_b128 v[136:139], v22 offset:0\n"
    "ds_read_b128 v[140:143], v23 offset:0\n"
    "ds_read_b128 v[144:147], v12 offset:512\n"
    "ds_read_b128 v[148:151], v21 offset:512\n"
    "ds_read_b128 v[152:155], v22 offset:512\n"
    "ds_read_b128 v[156:159], v23 offset:512\n"
    "ds_read_b128 v[160:163], v12 offset:4096\n"
    "ds_read_b128 v[164:167], v21 offset:4096\n"
    "ds_read_b128 v[168:171], v22 offset:4096\n"
    "ds_read_b128 v[172:175], v23 offset:4096\n"
    "ds_read_b128 v[176:179], v12 offset:4608\n"
    "ds_read_b128 v[180:183], v21 offset:4608\n"
    "ds_read_b128 v[184:187], v22 offset:4608\n"
    "ds_read_b128 v[188:191], v23 offset:4608\n"
    "v_mov_b32 v224, 0\n"
    "v_mov_b32 v225, 0\n"
    "v_mov_b32 v226, 0\n"
    "v_mov_b32 v227, 0\n"
    "v_mov_b32 v228, 0\n"
    "v_mov_b32 v229, 0\n"
    "v_mov_b32 v230, 0\n"
    "v_mov_b32 v231, 0\n"
    "v_mov_b32 v232, 0\n"
    "v_mov_b32 v233, 0\n"
    "v_mov_b32 v234, 0\n"
    "v_mov_b32 v235, 0\n"
    "v_mov_b32 v236, 0\n"
    "v_mov_b32 v237, 0\n"
    "v_mov_b32 v238, 0\n"
    "v_mov_b32 v239, 0\n"
    "v_mov_b32 v24, 0\n"
    "v_mov_b32 v25, 0\n"
    "v_mov_b32 v26, 0\n"
    "v_mov_b32 v27, 0\n"
    "v_mov_b32 v28, 0\n"
    "v_mov_b32 v29, 0\n"
    "v_mov_b32 v30, 0\n"
    "v_mov_b32 v31, 0\n"
    "v_mov_b32 v32, 0\n"
    "v_mov_b32 v33, 0\n"
    "v_mov_b32 v34, 0\n"
    "v_mov_b32 v35, 0\n"
    "v_mov_b32 v36, 0\n"
    "v_mov_b32 v37, 0\n"
    "v_mov_b32 v38, 0\n"
    "v_mov_b32 v39, 0\n"
    "s_cmp_lt_u32 s46, 4\n"
    "s_cbranch_scc0 SK37_%=\n"
    "s_waitcnt vmcnt(9)\n"
    "s_mov_b64 vcc, 0\n"
    "v_cmp_ne_u32 s[12:13], s44, v193\n"
    "s_or_b64 vcc, vcc, s[12:13]\n"
    "v_cmp_ne_u32 s[12:13], s44, v195\n"
    "s_or_b64 vcc, vcc, s[12:13]\n"
    "v_cmp_ne_u32 s[12:13], s44, v197\n"
    "s_or_b64 vcc, vcc, s[12:13]\n"
    "v_cmp_ne_u32 s[12:13], s44, v199\n"
    "s_or_b64 vcc, vcc, s[12:13]\n"
    "v_cmp_ne_u32 s[12:13], s44, v201\n"
    "s_or_b64 vcc, vcc, s[12:13]\n"
    "v_cmp_ne_u32 s[12:13], s44, v203\n"
    "s_or_b64 vcc, vcc, s[12:13]\n"
    "v_cmp_ne_u32 s[12:13], s44, v205\n"
    "s_or_b64 vcc, vcc, s[12:13]\n"
    "v_cmp_ne_u32 s[12:13], s44, v207\n"
    "s_or_b64 vcc, vcc, s[12:13]\n"
    "s_cbranch_vccz OK38_%=\n"
    "s_branch IN40_%=\n"
    "RT39_%=:\n"
    "s_sleep 2\n"
    "IN40_%=:\n"
    "s_add_i32 s64, s64, -1\n"
    "s_cmp_gt_i32 s64, 0\n"
    "s_cselect_b64 s[66:67], -1, 0\n"
    "buffer_load_dwordx4 v[192:195], v19, s[40:43], s60 offen sc1\n"
    "buffer_load_dwordx4 v[200:203], v19, s[40:43], s60 offen offset:1024 sc1\n"
    "buffer_load_dwordx4 v[196:199], v19, s[40:43], s61 offen sc1\n"
    "buffer_load_dwordx4 v[204:207], v19, s[40:43], s61 offen offset:1024 sc1\n"
    "s_waitcnt vmcnt(0)\n"
    "s_mov_b64 vcc, 0\n"
    "v_cmp_ne_u32 s[12:13], s44, v193\n"
    "s_or_b64 vcc, vcc, s[12:13]\n"
    "v_cmp_ne_u32 s[12:13], s44, v195\n"
    "s_or_b64 vcc, vcc, s[12:13]\n"
    "v_cmp_ne_u32 s[12:13], s44, v197\n"
    "s_or_b64 vcc, vcc, s[12:13]\n"
    "v_cmp_ne_u32 s[12:13], s44, v199\n"
    "s_or_b64 vcc, vcc, s[12:13]\n"
    "v_cmp_ne_u32 s[12:13], s44, v201\n"
    "s_or_b64 vcc, vcc, s[12:13]\n"
    "v_cmp_ne_u32 s[12:13], s44, v203\n"
    "s_or_b64 vcc, vcc, s[12:13]\n"
    "v_cmp_ne_u32 s[12:13], s44, v205\n"
    "s_or_b64 vcc, vcc, s[12:13]\n"
    "v_cmp_ne_u32 s[12:13], s44, v207\n"
    "s_or_b64 vcc, vcc, s[12:13]\n"
    "s_and_b64 vcc, vcc, s[66:67]\n"
    "s_cbranch_scc1 RT39_%=\n"
    "OK38_%=:\n"
    "v_add_f32 v16, v192, v196\n"
    "v_add_f32 v17, v194, v198\n"
    "v_add_f32 v10, v200, v204\n"
    "v_add_f32 v11, v202, v206\n"
    "v_mov_b32 v18, v16\n"
    "s_nop 1\n"
    "v_permlane32_swap_b32 v18, v16\n"
    "v_add_f32 v16, v18, v16\n"
    "v_mov_b32 v18, v16\n"
    "s_nop 1\n"
    "v_permlane16_swap_b32 v18, v16\n"
    "v_add_f32 v16, v18, v16\n"
    "v_mov_b32 v18, v17\n"
    "s_nop 1\n"
    "v_permlane32_swap_b32 v18, v17\n"
    "v_add_f32 v17, v18, v17\n"
    "v_mov_b32 v18, v17\n"
    "s_nop 1\n"
    "v_permlane16_swap_b32 v18, v17\n"
    "v_add_f32 v17, v18, v17\n"
    "v_mov_b32 v18, v10\n"
    "s_nop 1\n"
    "v_permlane32_swap_b32 v18, v10\n"
    "v_add_f32 v10, v18, v10\n"
    "v_mov_b32 v18, v10\n"
    "s_nop 1\n"
    "v_permlane16_swap_b32 v18, v10\n"
    "v_add_f32 v10, v18, v10\n"
    "v_mov_b32 v18, v11\n"
    "s_nop 1\n"
    "v_permlane32_swap_b32 v18, v11\n"
    "v_add_f32 v11, v18, v11\n"
    "v_mov_b32 v18, v11\n"
    "s_nop 1\n"
    "v_permlane16_swap_b32 v18, v11\n"
    "v_add_f32 v11, v18, v11\n"
    "v_mul_f32 v10, 0xbacccccd, v10\n"
    "v_mul_f32 v11, 0xbacccccd, v11\n"
    "v_cvt_pk_bf16_f32 v16, v16, v17\n"
    "v_and_b32 v17, 8, v0\n"
    "v_lshlrev_b32 v17, 2, v17\n"
    "v_and_b32 v18, 1, v0\n"
    "v_lshl_add_u32 v17, v18, 4, v17\n"
    "v_and_b32 v18, 6, v0\n"
    "v_lshl_add_u32 v17, v18, 1, v17\n"
    "s_lshl_b32 s3, s46, 6\n"
    "s_add_u32 s3, s3, 152320\n"
    "v_add_u32 v17, s3, v17\n"
    "ds_write_b32 v17, v16\n"
    "SK37_%=:\n"
    "s_cmp_eq_u32 s46, 4\n"
    "s_cbranch_scc0 SK41_%=\n"
    "s_waitcnt vmcnt(9)\n"
    "s_mov_b64 vcc, 0\n"
    "v_cmp_ne_u32 s[12:13], s44, v192\n"
    "s_or_b64 vcc, vcc, s[12:13]\n"
    "v_cmp_ne_u32 s[12:13], s45, v193\n"
    "s_or_b64 vcc, vcc, s[12:13]\n"
    "v_cmp_ne_u32 s[12:13], s44, v194\n"
    "s_or_b64 vcc, vcc, s[12:13]\n"
    "v_cmp_ne_u32 s[12:13], s45, v195\n"
    "s_or_b64 vcc, vcc, s[12:13]\n"
    "v_cmp_ne_u32 s[12:13], s44, v196\n"
    "s_or_b64 vcc, vcc, s[12:13]\n"
    "v_cmp_ne_u32 s[12:13], s45, v197\n"
    "s_or_b64 vcc, vcc, s[12:13]\n"
    "v_cmp_ne_u32 s[12:13], s44, v198\n"
    "s_or_b64 vcc, vcc, s[12:13]\n"
    "v_cmp_ne_u32 s[12:13], s45, v199\n"
    "s_or_b64 vcc, vcc, s[12:13]\n"
    "s_cbranch_vccz OK42_%=\n"
    "s_branch IN44_%=\n"
    "RT43_%=:\n"
    "s_sleep 2\n"
    "IN44_%=:\n"
    "s_add_i32 s64, s64, -1\n"
    "s_cmp_gt_i32 s64, 0\n"
    "s_cselect_b64 s[66:67], -1, 0\n"
    "buffer_load_dwordx4 v[192:195], v1, s[40:43], s60 offen sc1\n"
    "buffer_load_dwordx4 v[196:199], v1, s[40:43], s60 offen offset:1024 sc1\n"
    "s_waitcnt vmcnt(0)\n"
    "s_mov_b64 vcc, 0\n"
    "v_cmp_ne_u32 s[12:13], s44, v192\n"
    "s_or_b64 vcc, vcc, s[12:13]\n"
    "v_cmp_ne_u32 s[12:13], s45, v193\n"
    "s_or_b64 vcc, vcc, s[12:13]\n"
    "v_cmp_ne_u32 s[12:13], s44, v194\n"
    "s_or_b64 vcc, vcc, s[12:13]\n"
    "v_cmp_ne_u32 s[12:13], s45, v195\n"
    "s_or_b64 vcc, vcc, s[12:13]\n"
    "v_cmp_ne_u32 s[12:13], s44, v196\n"
    "s_or_b64 vcc, vcc, s[12:13]\n"
    "v_cmp_ne_u32 s[12:13], s45, v197\n"
    "s_or_b64 vcc, vcc, s[12:13]\n"
    "v_cmp_ne_u32 s[12:13], s44, v198\n"
    "s_or_b64 vcc, vcc, s[12:13]\n"
    "v_cmp_ne_u32 s[12:13], s45, v199\n"
    "s_or_b64 vcc, vcc, s[12:13]\n"
    "s_and_b64 vcc, vcc, s[66:67]\n"
    "s_cbranch_scc1 RT43_%=\n"
    "OK42_%=:\n"
    "SK41_%=:\n"
    "s_lshl_b32 s3, s46, 2\n"
    "s_add_u32 s3, s3, 152576\n"
    "v_mov_b32 v22, s3\n"
    "v_and_b32 v23, 7, v0\n"
    "v_lshlrev_b32 v23, 2, v23\n"
    "v_add_u32 v23, 151552, v23\n"
    "s_branch IN46_%=\n"
    "RT45_%=:\n"
    "s_sleep 1\n"
    "IN46_%=:\n"
    "s_add_i32 s64, s64, -1\n"
    "s_cmp_gt_i32 s64, 0\n"
    "s_cselect_b64 s[66:67], -1, 0\n"
    "ds_read_b128 v[240:243], v14\n"
    "ds_read_b128 v[244:247], v14 offset:128\n"
    "s_waitcnt lgkmcnt(0)\n"
    "v_max3_u32 v16, v240, v241, v242\n"
    "v_max3_u32 v16, v16, v243, v244\n"
    "v_max3_u32 v16, v16, v245, v246\n"
    "v_max_u32 v16, v16, v247\n"
    "v_cmp_eq_u32 vcc, -1, v16\n"
    "s_and_b64 vcc, vcc, s[66:67]\n"
    "s_cbranch_scc1 RT45_%=\n"
    "v_smfmac_f32_16x16x64_bf16 v[224:227], v[240:243], v[128:135], v3\n"
    "v_smfmac_f32_16x16x64_bf16 v[228:231], v[240:243], v[144:151], v3\n"
    "v_smfmac_f32_16x16x64_bf16 v[232:235], v[240:243], v[160:167], v3\n"
    "v_smfmac_f32_16x16x64_bf16 v[236:239], v[240:243], v[176:183], v3\n"
    "v_smfmac_f32_16x16x64_bf16 v[224:227], v[244:247], v[136:143], v3\n"
    "v_smfmac_f32_16x16x64_bf16 v[228:231], v[244:247], v[152:159], v3\n"
    "v_smfmac_f32_16x16x64_bf16 v[232:235], v[244:247], v[168:175], v3\n"
    "v_smfmac_f32_16x16x64_bf16 v[236:239], v[244:247], v[184:191], v3\n"
    "s_nop 1\n"
    "v_add_f32 v18, v224, v225\n"
    "s_nop 0\n"
    "v_add_f32 v19, v228, v229\n"
    "s_nop 0\n"
    "v_add_f32 v21, v232, v233\n"
    "v_max3_f32 v17, v18, v19, v21\n"
    "v_add_f32 v18, v236, v237\n"
    "v_max_f32 v17, v17, v18\n"
    "v_add_f32_dpp v16, v224, v225 quad_perm:[0,1,2,3] row_mask:0x1 bank_mask:0xf\n"
    "s_nop 0\n"
    "v_max_f32_dpp v17, v17, v17 row_ror:8 row_mask:0xf bank_mask:0xf\n"
    "v_add_f32_dpp v16, v228, v229 quad_perm:[0,1,2,3] row_mask:0x2 bank_mask:0xf\n"
    "s_nop 0\n"
    "v_max_f32_dpp v17, v17, v17 row_ror:4 row_mask:0xf bank_mask:0xf\n"
    "v_add_f32_dpp v16, v232, v233 quad_perm:[0,1,2,3] row_mask:0x4 bank_mask:0xf\n"
    "s_nop 0\n"
    "v_max_f32_dpp v17, v17, v17 row_ror:2 row_mask:0xf bank_mask:0xf\n"
    "v_add_f32_dpp v16, v236, v237 quad_perm:[0,1,2,3] row_mask:0x8 bank_mask:0xf\n"
    "s_nop 0\n"
    "v_max_f32_dpp v17, v17, v17 row_ror:1 row_mask:0xf bank_mask:0xf\n"
    "v_cvt_pk_bf16_f32 v18, v16, v17\n"
    "v_lshlrev_b32 v16, 16, v18\n"
    "v_and_b32 v17, 0xffff0000, v18\n"
    "v_pk_mul_f32 v[16:17], v[16:17], s[62:63] op_sel_hi:[1,0]\n"
    "v_cvt_pk_bf16_f32 v18, v16, v17\n"
    "v_lshlrev_b32 v16, 16, v18\n"
    "v_and_b32 v17, 0xffff0000, v18\n"
    "v_pk_mul_f32 v[16:17], v[16:17], s[62:63] op_sel:[0,1] op_sel_hi:[1,1]\n"
    "v_sub_f32 v18, v16, v17\n"
    "v_exp_f32 v18, v18\n"
    "s_mov_b64 exec, 1\n"
    "ds_write_b32 v22, v17\n"
    "s_mov_b64 exec, -1\n"
    "v_mov_b32_dpp v19, v18 quad_perm:[1,1,3,3] row_mask:0xf bank_mask:0xf\n"
    "v_cvt_pk_bf16_f32 v19, v18, v19\n"
    "s_nop 1\n"
    "v_mov_b32_dpp v240, v19 row_newbcast:0 row_mask:0xf bank_mask:0xf\n"
    "v_mov_b32_dpp v241, v19 row_newbcast:2 row_mask:0xf bank_mask:0xf\n"
    "v_mov_b32_dpp v242, v19 row_newbcast:4 row_mask:0xf bank_mask:0xf\n"
    "v_mov_b32_dpp v243, v19 row_newbcast:6 row_mask:0xf bank_mask:0xf\n"
    "v_mov_b32_dpp v244, v19 row_newbcast:8 row_mask:0xf bank_mask:0xf\n"
    "v_mov_b32_dpp v245, v19 row_newbcast:10 row_mask:0xf bank_mask:0xf\n"
    "v_mov_b32_dpp v246, v19 row_newbcast:12 row_mask:0xf bank_mask:0xf\n"
    "v_mov_b32_dpp v247, v19 row_newbcast:14 row_mask:0xf bank_mask:0xf\n"
    "v_add_f32_dpp v20, v18, v18 row_ror:8 row_mask:0xf bank_mask:0xf\n"
    "s_nop 1\n"
    "v_add_f32_dpp v20, v20, v20 row_ror:4 row_mask:0xf bank_mask:0xf\n"
    "s_nop 1\n"
    "v_add_f32_dpp v20, v20, v20 row_ror:2 row_mask:0xf bank_mask:0xf\n"
    "s_nop 1\n"
    "v_add_f32_dpp v20, v20, v20 row_ror:1 row_mask:0xf bank_mask:0xf\n"
    "s_nop 1\n"
    "v_add_f32_dpp v20, v20, v20 row_bcast:15 row_mask:0xa bank_mask:0xf\n"
    "s_nop 1\n"
    "v_add_f32_dpp v20, v20, v20 row_bcast:31 row_mask:0xc bank_mask:0xf\n"
    "s_lshl_b32 s3, s46, 9\n"
    "s_add_u32 s3, s3, 131072\n"
    "v_lshl_add_u32 v224, v0, 3, s3\n"
    "s_waitcnt vmcnt(15)\n"
    "v_mov_b32 v128, v64\n"
    "v_mov_b32 v136, v65\n"
    "v_mov_b32 v144, v66\n"
    "v_mov_b32 v152, v67\n"
    "s_waitcnt vmcnt(14)\n"
    "v_mov_b32 v129, v68\n"
    "v_mov_b32 v137, v69\n"
    "v_mov_b32 v145, v70\n"
    "v_mov_b32 v153, v71\n"
    "s_waitcnt vmcnt(13)\n"
    "v_mov_b32 v130, v72\n"
    "v_mov_b32 v138, v73\n"
    "v_mov_b32 v146, v74\n"
    "v_mov_b32 v154, v75\n"
    "s_waitcnt vmcnt(12)\n"
    "v_mov_b32 v131, v76\n"
    "v_mov_b32 v139, v77\n"
    "v_mov_b32 v147, v78\n"
    "v_mov_b32 v155, v79\n"
    "s_waitcnt vmcnt(11)\n"
    "v_mov_b32 v132, v80\n"
    "v_mov_b32 v140, v81\n"
    "v_mov_b32 v148, v82\n"
    "v_mov_b32 v156, v83\n"
    "s_waitcnt vmcnt(10)\n"
    "v_mov_b32 v133, v84\n"
    "v_mov_b32 v141, v85\n"
    "v_mov_b32 v149, v86\n"
    "v_mov_b32 v157, v87\n"
    "s_waitcnt vmcnt(9)\n"
    "v_mov_b32 v134, v88\n"
    "v_mov_b32 v142, v89\n"
    "v_mov_b32 v150, v90\n"
    "v_mov_b32 v158, v91\n"
    "s_waitcnt vmcnt(8)\n"
    "v_mov_b32 v135, v92\n"
    "v_mov_b32 v143, v93\n"
    "v_mov_b32 v151, v94\n"
    "v_mov_b32 v159, v95\n"
    "v_smfmac_f32_16x16x64_bf16 v[24:27], v[240:243], v[128:135], v2\n"
    "v_smfmac_f32_16x16x64_bf16 v[28:31], v[240:243], v[136:143], v2\n"
    "v_smfmac_f32_16x16x64_bf16 v[32:35], v[240:243], v[144:151], v2\n"
    "v_smfmac_f32_16x16x64_bf16 v[36:39], v[240:243], v[152:159], v2\n"
    "s_waitcnt vmcnt(7)\n"
    "v_mov_b32 v160, v96\n"
    "v_mov_b32 v168, v97\n"
    "v_mov_b32 v176, v98\n"
    "v_mov_b32 v184, v99\n"
    "s_waitcnt vmcnt(6)\n"
    "v_mov_b32 v161, v100\n"
    "v_mov_b32 v169, v101\n"
    "v_mov_b32 v177, v102\n"
    "v_mov_b32 v185, v103\n"
    "s_cmp_ge_u32 s46, 6\n"
    "s_cbranch_scc0 SK47_%=\n"
    "s_branch IN49_%=\n"
    "RT48_%=:\n"
    "IN49_%=:\n"
    "s_add_i32 s64, s64, -1\n"
    "s_cmp_gt_i32 s64, 0\n"
    "s_cselect_b64 s[66:67], -1, 0\n"
    "ds_read_b32 v21, v23 offset:1024\n"
    "v_and_b32 v225, 3, v0\n"
    "v_lshlrev_b32 v225, 7, v225\n"
    "v_and_b32 v226, 60, v0\n"
    "v_lshl_add_u32 v225, v226, 1, v225\n"
    "v_add_u32 v225, 131072, v225\n"
    "v_lshlrev_b32 v226, 2, v0\n"
    "s_mul_i32 s65, s51, 1408\n"
    "s_add_u32 s65, s65, 492160\n"
    "s_waitcnt lgkmcnt(0)\n"
    "v_cmp_eq_u32 vcc, -1, v21\n"
    "v_max_f32_dpp v16, v21, v21 row_ror:4 row_mask:0xf bank_mask:0xf\n"
    "s_and_b64 vcc, vcc, s[66:67]\n"
    "s_cbranch_scc1 RT48_%=\n"
    "v_max_f32_dpp v16, v16, v16 row_ror:2 row_mask:0xf bank_mask:0xf\n"
    "s_nop 1\n"
    "v_max_f32_dpp v16, v16, v16 row_ror:1 row_mask:0xf bank_mask:0xf\n"
    "v_sub_f32 v21, v21, v16\n"
    "v_exp_f32 v21, v21\n"
    "s_nop 0\n"
    "v_readlane_b32 s18, v21, 0\n"
    "v_readlane_b32 s19, v21, 1\n"
    "v_readlane_b32 s20, v21, 2\n"
    "v_readlane_b32 s21, v21, 3\n"
    "v_readlane_b32 s22, v21, 4\n"
    "v_readlane_b32 s23, v21, 5\n"
    "v_readlane_b32 s24, v21, 6\n"
    "v_readlane_b32 s25, v21, 7\n"
    "SK47_%=:\n"
    "s_waitcnt vmcnt(5)\n"
    "v_mov_b32 v162, v104\n"
    "v_mov_b32 v170, v105\n"
    "v_mov_b32 v178, v106\n"
    "v_mov_b32 v186, v107\n"
    "s_waitcnt vmcnt(4)\n"
    "v_mov_b32 v163, v108\n"
    "v_mov_b32 v171, v109\n"
    "v_mov_b32 v179, v110\n"
    "v_mov_b32 v187, v111\n"
    "s_waitcnt vmcnt(3)\n"
    "v_mov_b32 v164, v112\n"
    "v_mov_b32 v172, v113\n"
    "v_mov_b32 v180, v114\n"
    "v_mov_b32 v188, v115\n"
    "s_waitcnt vmcnt(2)\n"
    "v_mov_b32 v165, v116\n"
    "v_mov_b32 v173, v117\n"
    "v_mov_b32 v181, v118\n"
    "v_mov_b32 v189, v119\n"
    "s_waitcnt vmcnt(1)\n"
    "v_mov_b32 v166, v120\n"
    "v_mov_b32 v174, v121\n"
    "v_mov_b32 v182, v122\n"
    "v_mov_b32 v190, v123\n"
    "s_waitcnt vmcnt(0)\n"
    "v_mov_b32 v167, v124\n"
    "v_mov_b32 v175, v125\n"
    "v_mov_b32 v183, v126\n"
    "v_mov_b32 v191, v127\n"
    "v_smfmac_f32_16x16x64_bf16 v[24:27], v[244:247], v[160:167], v2\n"
    "v_smfmac_f32_16x16x64_bf16 v[28:31], v[244:247], v[168:175], v2\n"
    "v_smfmac_f32_16x16x64_bf16 v[32:35], v[244:247], v[176:183], v2\n"
    "v_smfmac_f32_16x16x64_bf16 v[36:39], v[244:247], v[184:191], v2\n"
    "s_nop 1\n"
    "v_mov_b32_dpp v18, v24 quad_perm:[0,1,2,3] row_mask:0x1 bank_mask:0xf\n"
    "v_mov_b32_dpp v19, v25 quad_perm:[0,1,2,3] row_mask:0x1 bank_mask:0xf\n"
    "v_mov_b32_dpp v18, v28 quad_perm:[0,1,2,3] row_mask:0x2 bank_mask:0xf\n"
    "v_mov_b32_dpp v19, v29 quad_perm:[0,1,2,3] row_mask:0x2 bank_mask:0xf\n"
    "v_mov_b32_dpp v18, v32 quad_perm:[0,1,2,3] row_mask:0x4 bank_mask:0xf\n"
    "v_mov_b32_dpp v19, v33 quad_perm:[0,1,2,3] row_mask:0x4 bank_mask:0xf\n"
    "v_mov_b32_dpp v18, v36 quad_perm:[0,1,2,3] row_mask:0x8 bank_mask:0xf\n"
    "v_mov_b32_dpp v19, v37 quad_perm:[0,1,2,3] row_mask:0x8 bank_mask:0xf\n"
    "ds_write_b64 v224, v[18:19]\n"
    "s_mov_b64 exec, s[56:57]\n"
    "ds_write_b32 v22, v20 offset:64\n"
    "s_mov_b64 exec, -1\n"
    "s_add_u32 s3, s52, 3\n"
    "s_cmp_lt_u32 s46, s3\n"
    "s_cbranch_scc0 N152_%=\n"
    "s_cmp_eq_u32 s52, 1\n"
    "s_cbranch_scc1 W150_%=\n"
    "s_cmp_eq_u32 s52, 2\n"
    "s_cbranch_scc1 M151_%=\n"
    "N152_%=:\n"
    "s_cmp_ge_u32 s46, 6\n"
    "s_cbranch_scc0 SK60_%=\n"
    "s_branch IN62_%=\n"
    "RT61_%=:\n"
    "IN62_%=:\n"
    "s_add_i32 s64, s64, -1\n"
    "s_cmp_gt_i32 s64, 0\n"
    "s_cselect_b64 s[66:67], -1, 0\n"
    "ds_read_b32 v17, v23 offset:1088\n"
    "s_waitcnt lgkmcnt(0)\n"
    "v_cmp_eq_u32 vcc, -1, v17\n"
    "s_and_b64 vcc, vcc, s[66:67]\n"
    "s_cbranch_scc1 RT61_%=\n"
    "SK60_%=:\n"
    "s_cmp_eq_u32 s46, 6\n"
    "s_cbranch_scc0 SK63_%=\n"
    "ds_read_b64 v[24:25], v225 offset:0\n"
    "ds_read_b64 v[26:27], v225 offset:512\n"
    "ds_read_b64 v[28:29], v225 offset:1024\n"
    "ds_read_b64 v[30:31], v225 offset:1536\n"
    "ds_read_b64 v[32:33], v225 offset:2048\n"
    "ds_read_b64 v[34:35], v225 offset:2560\n"
    "ds_read_b64 v[36:37], v225 offset:3072\n"
    "ds_read_b64 v[38:39], v225 offset:3584\n"
    "s_waitcnt lgkmcnt(5)\n"
    "v_pk_mul_f32 v[16:17], v[24:25], s[18:19] op_sel_hi:[1,0]\n"
    "v_pk_fma_f32 v[16:17], v[26:27], s[18:19], v[16:17] op_sel:[0,1,0] op_sel_hi:[1,1,1]\n"
    "v_pk_fma_f32 v[16:17], v[28:29], s[20:21], v[16:17] op_sel_hi:[1,0,1]\n"
    "s_waitcnt lgkmcnt(2)\n"
    "v_pk_fma_f32 v[16:17], v[30:31], s[20:21], v[16:17] op_sel:[0,1,0] op_sel_hi:[1,1,1]\n"
    "v_pk_fma_f32 v[16:17], v[32:33], s[22:23], v[16:17] op_sel_hi:[1,0,1]\n"
    "v_pk_fma_f32 v[16:17], v[34:35], s[22:23], v[16:17] op_sel:[0,1,0] op_sel_hi:[1,1,1]\n"
    "s_waitcnt lgkmcnt(0)\n"
    "v_pk_fma_f32 v[16:17], v[36:37], s[24:25], v[16:17] op_sel_hi:[1,0,1]\n"
    "v_pk_fma_f32 v[16:17], v[38:39], s[24:25], v[16:17] op_sel:[0,1,0] op_sel_hi:[1,1,1]\n"
    "v_cvt_pk_bf16_f32 v16, v16, v17\n"
    "buffer_store_dword v16, v226, s[40:43], s65 offen sc1\n"
    "ds_write_b32 v22, v22 offset:156\n"
    "SK63_%=:\n"
    "s_cmp_eq_u32 s46, 7\n"
    "s_cbranch_scc0 SK64_%=\n"
    "v_mul_f32 v17, v17, v21\n"
    "s_nop 1\n"
    "v_add_f32_dpp v17, v17, v17 row_ror:4 row_mask:0xf bank_mask:0xf\n"
    "s_nop 1\n"
    "v_add_f32_dpp v17, v17, v17 row_ror:2 row_mask:0xf bank_mask:0xf\n"
    "s_nop 1\n"
    "v_add_f32_dpp v17, v17, v17 row_ror:1 row_mask:0xf bank_mask:0xf\n"
    "s_mov_b64 exec, 1\n"
    "buffer_store_dwordx2 v[16:17], v0, s[40:43], s65 offen offset:256 sc1\n"
    "s_mov_b64 exec, -1\n"
    "ds_write_b32 v22, v22 offset:156\n"
    "SK64_%=:\n"
    "s_cmp_lg_u32 s52, 0\n"
    "s_cbranch_scc0 SK65_%=\n"
    "v_mov_b32 v16, 0\n"
    "v_lshlrev_b32 v17, 2, v0\n"
    "v_add_u32 v17, 143872, v17\n"
    "ds_write_b32 v17, v16\n"
    "ds_write_b32 v17, v16 offset:3072\n"
    "s_waitcnt lgkmcnt(0)\n"
    "s_barrier\n"
    "SK65_%=:\n"
    "s_branch Q253_%=\n"
    "W150_%=:\n"
    "s_cmp_ge_u32 s48, 10\n"
    "s_cselect_b32 s61, 1, 0\n"
    "s_mul_i32 s60, s61, 10\n"
    "s_sub_u32 s60, s48, s60\n"
    "s_lshl_b32 s60, s60, 7\n"
    "s_lshr_b32 s3, s46, 1\n"
    "s_lshl_b32 s61, s61, 1\n"
    "s_add_u32 s3, s3, s61\n"
    "s_lshl_b32 s3, s3, 3\n"
    "s_add_u32 s3, s3, s47\n"
    "s_lshl_b32 s3, s3, 1\n"
    "s_and_b32 s61, s46, 1\n"
    "s_add_u32 s3, s3, s61\n"
    "s_mul_i32 s3, s3, 81920\n"
    "s_add_u32 s3, s3, s60\n"
    "v_mul_u32_u24 v249, 5120, v15\n"
    "v_lshl_add_u32 v249, v13, 3, v249\n"
    "s_mov_b32 s54, 0\n"
    "s_mov_b32 s55, 20480\n"
    "s_mov_b32 s60, 40960\n"
    "s_mov_b32 s61, 61440\n"
    "s_add_u32 s4, s24, s3\n"
    "s_addc_u32 s5, s25, 0\n"
    "s_mov_b32 s6, 80768\n"
    "s_mov_b32 s7, 0x20000\n"
    "s_branch IN67_%=\n"
    "RT66_%=:\n"
    "s_sleep 2\n"
    "IN67_%=:\n"
    "s_add_i32 s64, s64, -1\n"
    "s_cmp_gt_i32 s64, 0\n"
    "s_cselect_b64 s[66:67], -1, 0\n"
    "ds_read_b32 v21, v23 offset:1120\n"
    "s_waitcnt lgkmcnt(0)\n"
    "v_cmp_eq_u32 vcc, -1, v21\n"
    "s_and_b64 vcc, vcc, s[66:67]\n"
    "s_cbranch_scc1 RT66_%=\n"
    "buffer_load_dwordx2 v[192:193], v249, s[4:7], s54 offen offset:0 nt\n"
    "buffer_load_dwordx2 v[194:195], v249, s[4:7], s54 offen offset:1280 nt\n"
    "buffer_load_dwordx2 v[196:197], v249, s[4:7], s54 offen offset:2560 nt\n"
    "buffer_load_dwordx2 v[198:199], v249, s[4:7], s54 offen offset:3840 nt\n"
    "buffer_load_dwordx2 v[200:201], v249, s[4:7], s55 offen offset:0 nt\n"
    "buffer_load_dwordx2 v[202:203], v249, s[4:7], s55 offen offset:1280 nt\n"
    "buffer_load_dwordx2 v[204:205], v249, s[4:7], s55 offen offset:2560 nt\n"
    "buffer_load_dwordx2 v[206:207], v249, s[4:7], s55 offen offset:3840 nt\n"
    "v_mov_b32 v16, 152756\n"
    "s_branch IN69_%=\n"
    "RT68_%=:\n"
    "s_sleep 1\n"
    "IN69_%=:\n"
    "s_add_i32 s64, s64, -1\n"
    "s_cmp_gt_i32 s64, 0\n"
    "s_cselect_b64 s[66:67], -1, 0\n"
    "ds_read2_b32 v[18:19], v16 offset1:1\n"
    "s_waitcnt lgkmcnt(0)\n"
    "v_min_u32 v18, v18, v19\n"
    "v_cmp_eq_u32 vcc, 0, v18\n"
    "s_and_b64 vcc, vcc, s[66:67]\n"
    "s_cbranch_scc1 RT68_%=\n"
    "buffer_load_dwordx2 v[208:209], v249, s[4:7], s60 offen offset:0 nt\n"
    "buffer_load_dwordx2 v[210:211], v249, s[4:7], s60 offen offset:1280 nt\n"
    "buffer_load_dwordx2 v[212:213], v249, s[4:7], s60 offen offset:2560 nt\n"
    "buffer_load_dwordx2 v[214:215], v249, s[4:7], s60 offen offset:3840 nt\n"
    "buffer_load_dwordx2 v[216:217], v249, s[4:7], s61 offen offset:0 nt\n"
    "buffer_load_dwordx2 v[218:219], v249, s[4:7], s61 offen offset:1280 nt\n"
    "buffer_load_dwordx2 v[220:221], v249, s[4:7], s61 offen offset:2560 nt\n"
    "buffer_load_dwordx2 v[222:223], v249, s[4:7], s61 offen offset:3840 nt\n"
    "v_lshrrev_b32 v16, 2, v13\n"
    "v_lshlrev_b32 v16, 1, v16\n"
    "v_and_b32 v17, 1, v0\n"
    "v_add_u32 v16, v16, v17\n"
    "v_mul_u32_u24 v88, 1408, v16\n"
    "v_and_b32 v17, 2, v0\n"
    "v_lshl_add_u32 v16, v17, 1, v15\n"
    "v_lshl_add_u32 v88, v16, 4, v88\n"
    "v_lshrrev_b32 v17, 1, v17\n"
    "v_mul_u32_u24 v90, 0x5555, v17\n"
    "v_add_u32 v90, 0x8888, v90\n"
    "v_lshrrev_b32 v16, 3, v0\n"
    "v_mul_u32_u24 v89, 1408, v16\n"
    "s_lshr_b32 s3, s46, 1\n"
    "s_lshl_b32 s3, s3, 3\n"
    "s_lshl_b32 s60, s47, 5\n"
    "s_add_u32 s3, s3, s60\n"
    "s_cmp_ge_u32 s48, 10\n"
    "s_cselect_b32 s60, 16, 0\n"
    "s_add_u32 s3, s3, s60\n"
    "s_mul_i32 s61, s3, 1408\n"
    "s_add_u32 s61, s61, 492160\n"
    "s_and_b32 s3, s46, 1\n"
    "s_lshl_b32 s3, s3, 7\n"
    "s_add_u32 s60, s61, s3\n"
    "s_lshl_b32 s3, s46, 8\n"
    "s_add_u32 s3, s3, 141312\n"
    "v_lshl_add_u32 v91, v15, 2, s3\n"
    "v_lshl_add_u32 v91, v13, 4, v91\n"
    "v_and_b32 v17, 31, v0\n"
    "v_lshlrev_b32 v92, 3, v17\n"
    "v_add_u32 v92, 141312, v92\n"
    "v_lshrrev_b32 v16, 5, v0\n"
    "s_cmp_ge_u32 s48, 10\n"
    "s_cselect_b32 s65, 1, 0\n"
    "s_mul_i32 s3, s65, 10\n"
    "s_sub_u32 s3, s48, s3\n"
    "s_lshl_b32 s3, s3, 7\n"
    "s_lshl_b32 s65, s65, 3\n"
    "s_add_u32 s65, s65, s47\n"
    "s_mul_i32 s65, s65, 1280\n"
    "s_add_u32 s65, s65, s3\n"
    "v_mul_u32_u24 v93, 20480, v16\n"
    "v_lshl_add_u32 v93, v17, 2, v93\n"
    "s_mul_i32 s3, s46, 40960\n"
    "s_add_u32 s65, s65, s3\n"
    "s_add_u32 s65, s65, 163840\n"
    "v_mov_b32 v64, -1\n"
    "v_mov_b32 v65, -1\n"
    "v_mov_b32 v66, -1\n"
    "v_mov_b32 v67, -1\n"
    "v_mov_b32 v68, -1\n"
    "v_mov_b32 v69, -1\n"
    "s_mov_b32 s26, 1\n"
    "s_mov_b64 s[66:67], -1\n"
    "s_sleep 18\n"
    "buffer_load_dwordx4 v[64:67], v88, s[40:43], s60 offen sc1\n"
    "buffer_load_dwordx2 v[68:69], v89, s[40:43], s61 offen offset:256 sc1\n"
    "s_waitcnt vmcnt(2)\n"
    "v_perm_b32 v224, v208, v192, s38\n"
    "v_perm_b32 v232, v208, v192, s39\n"
    "v_perm_b32 v240, v209, v193, s38\n"
    "v_perm_b32 v248, v209, v193, s39\n"
    "v_perm_b32 v225, v210, v194, s38\n"
    "v_perm_b32 v233, v210, v194, s39\n"
    "v_perm_b32 v241, v211, v195, s38\n"
    "v_perm_b32 v249, v211, v195, s39\n"
    "v_perm_b32 v226, v212, v196, s38\n"
    "v_perm_b32 v234, v212, v196, s39\n"
    "v_perm_b32 v242, v213, v197, s38\n"
    "v_perm_b32 v250, v213, v197, s39\n"
    "v_perm_b32 v227, v214, v198, s38\n"
    "v_perm_b32 v235, v214, v198, s39\n"
    "v_perm_b32 v243, v215, v199, s38\n"
    "v_perm_b32 v251, v215, v199, s39\n"
    "v_perm_b32 v228, v216, v200, s38\n"
    "v_perm_b32 v236, v216, v200, s39\n"
    "v_perm_b32 v244, v217, v201, s38\n"
    "v_perm_b32 v252, v217, v201, s39\n"
    "v_perm_b32 v229, v218, v202, s38\n"
    "v_perm_b32 v237, v218, v202, s39\n"
    "v_perm_b32 v245, v219, v203, s38\n"
    "v_perm_b32 v253, v219, v203, s39\n"
    "v_perm_b32 v230, v220, v204, s38\n"
    "v_perm_b32 v238, v220, v204, s39\n"
    "v_perm_b32 v246, v221, v205, s38\n"
    "v_perm_b32 v254, v221, v205, s39\n"
    "v_perm_b32 v231, v222, v206, s38\n"
    "v_perm_b32 v239, v222, v206, s39\n"
    "v_perm_b32 v247, v223, v207, s38\n"
    "v_perm_b32 v255, v223, v207, s39\n"
    "v_mov_b32 v72, 0\n"
    "v_mov_b32 v73, 0\n"
    "v_mov_b32 v74, 0\n"
    "v_mov_b32 v75, 0\n"
    "v_mov_b32 v76, 0\n"
    "v_mov_b32 v77, 0\n"
    "v_mov_b32 v78, 0\n"
    "v_mov_b32 v79, 0\n"
    "v_mov_b32 v80, 0\n"
    "v_mov_b32 v81, 0\n"
    "v_mov_b32 v82, 0\n"
    "v_mov_b32 v83, 0\n"
    "v_mov_b32 v84, 0\n"
    "v_mov_b32 v85, 0\n"
    "v_mov_b32 v86, 0\n"
    "v_mov_b32 v87, 0\n"
    "s_waitcnt vmcnt(0)\n"
    "s_branch CHK72_%=\n"
    "RT70_%=:\n"
    "s_sub_u32 s26, s26, 1\n"
    "s_cmp_lt_u32 s26, 0\n"
    "s_cbranch_scc0 NS73_%=\n"
    "s_sleep 6\n"
    "NS73_%=:\n"
    "s_mov_b64 exec, s[18:19]\n"
    "buffer_load_dwordx4 v[64:67], v88, s[40:43], s60 offen sc1\n"
    "s_mov_b64 exec, s[20:21]\n"
    "buffer_load_dwordx2 v[68:69], v89, s[40:43], s61 offen offset:256 sc1\n"
    "s_mov_b64 exec, -1\n"
    "s_add_i32 s64, s64, -1\n"
    "s_cmp_gt_i32 s64, 0\n"
    "s_cselect_b64 s[66:67], -1, 0\n"
    "MID71_%=:\n"
    "v_mov_b32 v72, 0\n"
    "v_mov_b32 v73, 0\n"
    "v_mov_b32 v74, 0\n"
    "v_mov_b32 v75, 0\n"
    "v_mov_b32 v76, 0\n"
    "v_mov_b32 v77, 0\n"
    "v_mov_b32 v78, 0\n"
    "v_mov_b32 v79, 0\n"
    "v_mov_b32 v80, 0\n"
    "v_mov_b32 v81, 0\n"
    "v_mov_b32 v82, 0\n"
    "v_mov_b32 v83, 0\n"
    "v_mov_b32 v84, 0\n"
    "v_mov_b32 v85, 0\n"
    "v_mov_b32 v86, 0\n"
    "v_mov_b32 v87, 0\n"
    "s_waitcnt vmcnt(0)\n"
    "CHK72_%=:\n"
    "v_max3_u32 v18, v64, v65, v66\n"
    "v_max_u32 v18, v18, v67\n"
    "v_max_u32 v19, v68, v69\n"
    "v_smfmac_f32_16x16x64_bf16 v[72:75], v[64:67], v[224:231], v90\n"
    "v_max_f32_dpp v16, v68, v68 row_ror:8 row_mask:0xf bank_mask:0xf\n"
    "v_cmp_eq_u32 s[18:19], -1, v18\n"
    "v_mov_b32 v17, v16\n"
    "v_smfmac_f32_16x16x64_bf16 v[76:79], v[64:67], v[232:239], v90\n"
    "v_cmp_eq_u32 s[20:21], -1, v19\n"
    "v_permlane16_swap_b32 v17, v16\n"
    "v_max_f32 v16, v16, v17\n"
    "v_smfmac_f32_16x16x64_bf16 v[80:83], v[64:67], v[240:247], v90\n"
    "v_mov_b32 v17, v16\n"
    "s_or_b64 vcc, s[18:19], s[20:21]\n"
    "v_smfmac_f32_16x16x64_bf16 v[84:87], v[64:67], v[248:255], v90\n"
    "s_and_b64 vcc, vcc, s[66:67]\n"
    "s_cbranch_scc1 RT70_%=\n"
    "v_permlane32_swap_b32 v17, v16\n"
    "v_max_f32 v16, v16, v17\n"
    "v_sub_f32 v16, v68, v16\n"
    "v_exp_f32 v16, v16\n"
    "v_pk_add_f32 v[72:73], v[72:73], v[74:75]\n"
    "v_mul_f32 v17, v16, v69\n"
    "v_pk_add_f32 v[76:77], v[76:77], v[78:79]\n"
    "v_mul_f32_dpp v72, v16, v72 row_newbcast:0 row_mask:0xf bank_mask:0xf\n"
    "v_add_f32_dpp v17, v17, v17 row_ror:8 row_mask:0xf bank_mask:0xf\n"
    "v_fmac_f32_dpp v72, v16, v73 row_newbcast:8 row_mask:0xf bank_mask:0xf\n"
    "v_mov_b32 v18, v17\n"
    "v_pk_add_f32 v[80:81], v[80:81], v[82:83]\n"
    "v_mul_f32_dpp v76, v16, v76 row_newbcast:0 row_mask:0xf bank_mask:0xf\n"
    "v_permlane16_swap_b32 v18, v17\n"
    "v_add_f32 v17, v17, v18\n"
    "v_pk_add_f32 v[84:85], v[84:85], v[86:87]\n"
    "v_mov_b32 v18, v17\n"
    "v_mul_f32_dpp v80, v16, v80 row_newbcast:0 row_mask:0xf bank_mask:0xf\n"
    "v_fmac_f32_dpp v76, v16, v77 row_newbcast:8 row_mask:0xf bank_mask:0xf\n"
    "v_permlane32_swap_b32 v18, v17\n"
    "v_fmac_f32_dpp v80, v16, v81 row_newbcast:8 row_mask:0xf bank_mask:0xf\n"
    "v_mul_f32_dpp v84, v16, v84 row_newbcast:0 row_mask:0xf bank_mask:0xf\n"
    "v_fmac_f32_dpp v84, v16, v85 row_newbcast:8 row_mask:0xf bank_mask:0xf\n"
    "v_add_f32 v17, v17, v18\n"
    "v_permlane32_swap_b32 v72, v80\n"
    "v_rcp_f32 v17, v17\n"
    "v_permlane32_swap_b32 v76, v84\n"
    "v_add_f32 v72, v72, v80\n"
    "v_add_f32 v76, v76, v84\n"
    "s_nop 1\n"
    "v_permlane16_swap_b32 v72, v76\n"
    "v_add_f32 v72, v72, v76\n"
    "v_mul_f32 v72, v72, v17\n"
    "ds_write_b32 v91, v72\n"
    "s_waitcnt lgkmcnt(0)\n"
    "s_barrier\n"
    "s_cmp_lt_u32 s46, 4\n"
    "s_cbranch_scc0 SK74_%=\n"
    "ds_read_b64 v[72:73], v92 offset:0\n"
    "ds_read_b64 v[74:75], v92 offset:256\n"
    "ds_read_b64 v[76:77], v92 offset:512\n"
    "ds_read_b64 v[78:79], v92 offset:768\n"
    "s_waitcnt lgkmcnt(2)\n"
    "v_pk_add_f32 v[72:73], v[72:73], v[74:75]\n"
    "s_waitcnt lgkmcnt(0)\n"
    "v_pk_add_f32 v[76:77], v[76:77], v[78:79]\n"
    "v_pk_add_f32 v[72:73], v[72:73], v[76:77]\n"
    "v_cvt_pk_bf16_f32 v72, v72, v73\n"
    "buffer_store_dword v72, v93, s[40:43], s65 offen sc1\n"
    "SK74_%=:\n"
    "s_waitcnt vmcnt(0)\n"
    "s_branch Q253_%=\n"
    "M151_%=:\n"
    "s_cmp_eq_u32 s46, 4\n"
    "s_cbranch_scc0 SK75_%=\n"
    "s_setprio 3\n"
    "SK75_%=:\n"
    "s_mov_b32 s12, s28\n"
    "s_mov_b32 s13, s29\n"
    "s_mov_b32 s14, 3456\n"
    "s_mov_b32 s15, 0x20000\n"
    "v_and_b32 v16, 7, v0\n"
    "v_lshl_add_u32 v16, v15, 3, v16\n"
    "v_lshlrev_b32 v16, 1, v16\n"
    "s_lshl_b32 s3, s53, 6\n"
    "v_mov_b32 v98, 0\n"
    "buffer_load_short_d16_hi v98, v16, s[12:15], s3 offen\n"
    "s_mul_i32 s3, s46, 442368\n"
    "s_lshl_b32 s60, s53, 6\n"
    "s_add_u32 s3, s3, s60\n"
    "s_add_u32 s4, s26, s3\n"
    "s_addc_u32 s5, s27, 0\n"
    "s_mov_b32 s6, 438976\n"
    "s_mov_b32 s7, 0x20000\n"
    "s_mul_i32 s3, s53, 40960\n"
    "s_lshl_b32 s60, s46, 8\n"
    "s_add_u32 s3, s3, s60\n"
    "s_add_u32 s8, s30, s3\n"
    "s_addc_u32 s9, s31, 0\n"
    "s_mov_b32 s10, 39936\n"
    "s_mov_b32 s11, 0x20000\n"
    "s_mov_b32 s12, s16\n"
    "s_mov_b32 s13, s17\n"
    "s_mov_b32 s14, 1280\n"
    "s_mov_b32 s15, 0x20000\n"
    "v_and_b32 v249, 1, v15\n"
    "v_lshlrev_b32 v249, 4, v249\n"
    "v_and_b32 v250, 2, v15\n"
    "v_add_u32 v249, v249, v250\n"
    "v_mul_u32_u24 v249, 3456, v249\n"
    "v_lshl_add_u32 v249, v13, 2, v249\n"
    "v_mul_u32_u24 v250, 5120, v15\n"
    "v_lshl_add_u32 v250, v13, 4, v250\n"
    "v_lshlrev_b32 v251, 2, v15\n"
    "v_lshl_add_u32 v251, v13, 4, v251\n"
    "v_add_u32 v251, s60, v251\n"
    "s_and_b32 s3, s53, 1\n"
    "s_mul_i32 s54, s3, 221184\n"
    "s_lshl_b32 s55, s54, 1\n"
    "s_sub_u32 s55, 69120, s55\n"
    "s_lshl_b32 s3, s3, 7\n"
    "v_xor_b32 v251, s3, v251\n"
    "s_mov_b32 s60, 0\n"
    "s_mov_b32 s61, 20480\n"
    "s_branch IN77_%=\n"
    "RT76_%=:\n"
    "s_sleep 2\n"
    "IN77_%=:\n"
    "s_add_i32 s64, s64, -1\n"
    "s_cmp_gt_i32 s64, 0\n"
    "s_cselect_b64 s[66:67], -1, 0\n"
    "ds_read_b32 v21, v23 offset:1120\n"
    "s_waitcnt lgkmcnt(0)\n"
    "v_cmp_eq_u32 vcc, -1, v21\n"
    "s_and_b64 vcc, vcc, s[66:67]\n"
    "s_cbranch_scc1 RT76_%=\n"
    "buffer_load_dword v252, v251, s[12:15], 0 offen\n"
    "buffer_load_dword v192, v249, s[4:7], s54 offen offset:0 nt\n"
    "buffer_load_dword v193, v249, s[4:7], s54 offen offset:3456 nt\n"
    "s_add_u32 s54, s54, 13824\n"
    "buffer_load_dword v196, v249, s[4:7], s54 offen offset:0 nt\n"
    "buffer_load_dword v197, v249, s[4:7], s54 offen offset:3456 nt\n"
    "s_add_u32 s54, s54, 13824\n"
    "buffer_load_dword v194, v249, s[4:7], s54 offen offset:0 nt\n"
    "buffer_load_dword v195, v249, s[4:7], s54 offen offset:3456 nt\n"
    "s_add_u32 s54, s54, 13824\n"
    "buffer_load_dword v198, v249, s[4:7], s54 offen offset:0 nt\n"
    "buffer_load_dword v199, v249, s[4:7], s54 offen offset:3456 nt\n"
    "s_add_u32 s54, s54, 69120\n"
    "buffer_load_dword v200, v249, s[4:7], s54 offen offset:0 nt\n"
    "buffer_load_dword v201, v249, s[4:7], s54 offen offset:3456 nt\n"
    "s_add_u32 s54, s54, 13824\n"
    "buffer_load_dword v204, v249, s[4:7], s54 offen offset:0 nt\n"
    "buffer_load_dword v205, v249, s[4:7], s54 offen offset:3456 nt\n"
    "s_add_u32 s54, s54, 13824\n"
    "buffer_load_dword v202, v249, s[4:7], s54 offen offset:0 nt\n"
    "buffer_load_dword v203, v249, s[4:7], s54 offen offset:3456 nt\n"
    "s_add_u32 s54, s54, 13824\n"
    "buffer_load_dword v206, v249, s[4:7], s54 offen offset:0 nt\n"
    "buffer_load_dword v207, v249, s[4:7], s54 offen offset:3456 nt\n"
    "s_add_u32 s54, s54, s55\n"
    "v_mov_b32 v16, 152756\n"
    "s_branch IN79_%=\n"
    "RT78_%=:\n"
    "s_sleep 1\n"
    "IN79_%=:\n"
    "s_add_i32 s64, s64, -1\n"
    "s_cmp_gt_i32 s64, 0\n"
    "s_cselect_b64 s[66:67], -1, 0\n"
    "ds_read2_b32 v[18:19], v16 offset1:1\n"
    "s_waitcnt lgkmcnt(0)\n"
    "v_min_u32 v18, v18, v19\n"
    "v_cmp_eq_u32 vcc, 0, v18\n"
    "s_and_b64 vcc, vcc, s[66:67]\n"
    "s_cbranch_scc1 RT78_%=\n"
    "buffer_load_dwordx4 v[64:67], v250, s[8:11], s60 offen offset:0 nt\n"
    "buffer_load_dwordx4 v[68:71], v250, s[8:11], s60 offen offset:1280 nt\n"
    "buffer_load_dwordx4 v[72:75], v250, s[8:11], s60 offen offset:2560 nt\n"
    "buffer_load_dwordx4 v[76:79], v250, s[8:11], s60 offen offset:3840 nt\n"
    "buffer_load_dwordx4 v[80:83], v250, s[8:11], s61 offen offset:0 nt\n"
    "buffer_load_dwordx4 v[84:87], v250, s[8:11], s61 offen offset:1280 nt\n"
    "buffer_load_dwordx4 v[88:91], v250, s[8:11], s61 offen offset:2560 nt\n"
    "buffer_load_dwordx4 v[92:95], v250, s[8:11], s61 offen offset:3840 nt\n"
    "buffer_load_dword v208, v249, s[4:7], s54 offen offset:0 nt\n"
    "buffer_load_dword v209, v249, s[4:7], s54 offen offset:3456 nt\n"
    "s_add_u32 s54, s54, 13824\n"
    "buffer_load_dword v212, v249, s[4:7], s54 offen offset:0 nt\n"
    "buffer_load_dword v213, v249, s[4:7], s54 offen offset:3456 nt\n"
    "s_add_u32 s54, s54, 13824\n"
    "buffer_load_dword v210, v249, s[4:7], s54 offen offset:0 nt\n"
    "buffer_load_dword v211, v249, s[4:7], s54 offen offset:3456 nt\n"
    "s_add_u32 s54, s54, 13824\n"
    "buffer_load_dword v214, v249, s[4:7], s54 offen offset:0 nt\n"
    "buffer_load_dword v215, v249, s[4:7], s54 offen offset:3456 nt\n"
    "s_add_u32 s54, s54, 69120\n"
    "buffer_load_dword v216, v249, s[4:7], s54 offen offset:0 nt\n"
    "buffer_load_dword v217, v249, s[4:7], s54 offen offset:3456 nt\n"
    "s_add_u32 s54, s54, 13824\n"
    "buffer_load_dword v220, v249, s[4:7], s54 offen offset:0 nt\n"
    "buffer_load_dword v221, v249, s[4:7], s54 offen offset:3456 nt\n"
    "s_add_u32 s54, s54, 13824\n"
    "buffer_load_dword v218, v249, s[4:7], s54 offen offset:0 nt\n"
    "buffer_load_dword v219, v249, s[4:7], s54 offen offset:3456 nt\n"
    "s_add_u32 s54, s54, 13824\n"
    "buffer_load_dword v222, v249, s[4:7], s54 offen offset:0 nt\n"
    "buffer_load_dword v223, v249, s[4:7], s54 offen offset:3456 nt\n"
    "s_add_u32 s54, s54, 69120\n"
    "s_setprio 0\n"
    "s_and_b32 s3, s53, 1\n"
    "s_lshl_b32 s3, s3, 3\n"
    "v_xor_b32 v16, s3, v13\n"
    "v_mul_u32_u24 v99, 1280, v15\n"
    "v_lshl_add_u32 v99, v16, 4, v99\n"
    "s_and_b32 s60, s47, 7\n"
    "s_mul_i32 s60, s60, 20480\n"
    "s_lshl_b32 s3, s46, 8\n"
    "s_add_u32 s60, s60, s3\n"
    "s_add_u32 s60, s60, 163840\n"
    "s_add_u32 s61, s60, 5120\n"
    "s_add_u32 s4, s60, 10240\n"
    "s_add_u32 s5, s60, 15360\n"
    "v_lshlrev_b32 v100, 2, v15\n"
    "v_lshl_add_u32 v100, v16, 4, v100\n"
    "s_add_u32 s55, s3, 2690560\n"
    "s_mul_i32 s54, s53, 71680\n"
    "s_cmp_lt_u32 s53, 8\n"
    "s_cselect_b32 s54, s54, 0x40000000\n"
    "v_add_u32 v100, s54, v100\n"
    "s_lshl_b32 s54, s46, 3\n"
    "s_add_u32 s54, s54, 152704\n"
    "v_lshrrev_b32 v101, 5, v0\n"
    "v_lshl_add_u32 v101, v101, 2, s54\n"
    "v_lshrrev_b32 v17, 3, v13\n"
    "v_mul_u32_u24 v102, 1536, v17\n"
    "v_lshl_add_u32 v102, v15, 6, v102\n"
    "v_and_b32 v16, 7, v0\n"
    "v_lshl_add_u32 v102, v16, 3, v102\n"
    "v_add_u32 v102, 141312, v102\n"
    "v_lshlrev_b32 v103, 4, v17\n"
    "v_add_u32 v103, 152704, v103\n"
    "v_lshlrev_b32 v124, 3, v17\n"
    "v_add_u32 v124, 152736, v124\n"
    "s_lshl_b32 s54, s46, 9\n"
    "s_add_u32 s54, s54, 141312\n"
    "v_add_u32 v16, 1, v15\n"
    "v_and_b32 v16, 2, v16\n"
    "v_lshl_add_u32 v125, v16, 7, s54\n"
    "v_lshl_add_u32 v125, v13, 4, v125\n"
    "v_and_b32 v16, 1, v15\n"
    "v_lshl_add_u32 v125, v16, 2, v125\n"
    "v_mul_u32_u24 v104, 71680, v15\n"
    "v_lshl_add_u32 v104, v13, 4, v104\n"
    "s_mul_i32 s65, s53, 1280\n"
    "s_add_u32 s65, s65, s3\n"
    "s_add_u32 s65, s65, 2621440\n"
    "s_add_u32 s27, s65, 286720\n"
    "v_mov_b32 v105, 0x3f803f80\n"
    "v_mov_b32 v106, 0xc0135761\n"
    "v_mov_b32 v107, 0x2edbe6ff\n"
    "v_mov_b32 v24, -1\n"
    "v_mov_b32 v25, -1\n"
    "v_mov_b32 v26, -1\n"
    "v_mov_b32 v27, -1\n"
    "v_mov_b32 v28, -1\n"
    "v_mov_b32 v29, -1\n"
    "v_mov_b32 v30, -1\n"
    "v_mov_b32 v31, -1\n"
    "v_mov_b32 v32, -1\n"
    "v_mov_b32 v33, -1\n"
    "v_mov_b32 v34, -1\n"
    "v_mov_b32 v35, -1\n"
    "v_mov_b32 v36, -1\n"
    "v_mov_b32 v37, -1\n"
    "v_mov_b32 v38, -1\n"
    "v_mov_b32 v39, -1\n"
    "s_mov_b32 s26, 1\n"
    "s_mov_b64 s[66:67], -1\n"
    "s_sleep 12\n"
    "buffer_load_dwordx4 v[24:27], v99, s[40:43], s60 offen sc1\n"
    "buffer_load_dwordx4 v[28:31], v99, s[40:43], s61 offen sc1\n"
    "buffer_load_dwordx4 v[32:35], v99, s[40:43], s4 offen sc1\n"
    "buffer_load_dwordx4 v[36:39], v99, s[40:43], s5 offen sc1\n"
    "s_waitcnt vmcnt(4)\n"
    "v_lshlrev_b32 v96, 16, v252\n"
    "v_and_b32 v97, 0xffff0000, v252\n"
    "v_mov_b32 v224, v64\n"
    "v_mov_b32 v232, v65\n"
    "v_mov_b32 v240, v66\n"
    "v_mov_b32 v248, v67\n"
    "v_mov_b32 v225, v68\n"
    "v_mov_b32 v233, v69\n"
    "v_mov_b32 v241, v70\n"
    "v_mov_b32 v249, v71\n"
    "v_mov_b32 v226, v72\n"
    "v_mov_b32 v234, v73\n"
    "v_mov_b32 v242, v74\n"
    "v_mov_b32 v250, v75\n"
    "v_mov_b32 v227, v76\n"
    "v_mov_b32 v235, v77\n"
    "v_mov_b32 v243, v78\n"
    "v_mov_b32 v251, v79\n"
    "v_mov_b32 v228, v80\n"
    "v_mov_b32 v236, v81\n"
    "v_mov_b32 v244, v82\n"
    "v_mov_b32 v252, v83\n"
    "v_mov_b32 v229, v84\n"
    "v_mov_b32 v237, v85\n"
    "v_mov_b32 v245, v86\n"
    "v_mov_b32 v253, v87\n"
    "v_mov_b32 v230, v88\n"
    "v_mov_b32 v238, v89\n"
    "v_mov_b32 v246, v90\n"
    "v_mov_b32 v254, v91\n"
    "v_mov_b32 v231, v92\n"
    "v_mov_b32 v239, v93\n"
    "v_mov_b32 v247, v94\n"
    "v_mov_b32 v255, v95\n"
    "v_mov_b32 v76, v105\n"
    "v_mov_b32 v77, v105\n"
    "v_mov_b32 v78, v105\n"
    "v_mov_b32 v79, v105\n"
    "v_mov_b32 v80, v105\n"
    "v_mov_b32 v81, v105\n"
    "v_mov_b32 v82, v105\n"
    "v_mov_b32 v83, v105\n"
    "v_mov_b32 v84, v105\n"
    "v_mov_b32 v85, v105\n"
    "v_mov_b32 v86, v105\n"
    "v_mov_b32 v87, v105\n"
    "v_mov_b32 v88, v105\n"
    "v_mov_b32 v89, v105\n"
    "v_mov_b32 v90, v105\n"
    "v_mov_b32 v91, v105\n"
    "v_mov_b32 v92, 0\n"
    "v_mov_b32 v93, 0\n"
    "v_mov_b32 v94, 0\n"
    "v_mov_b32 v95, 0\n"
    "v_mov_b32 v74, 0\n"
    "v_mov_b32 v75, 0\n"
    "v_mov_b32 v108, 0\n"
    "v_mov_b32 v109, 0\n"
    "v_mov_b32 v110, 0\n"
    "v_mov_b32 v111, 0\n"
    "v_mov_b32 v112, 0\n"
    "v_mov_b32 v113, 0\n"
    "v_mov_b32 v114, 0\n"
    "v_mov_b32 v115, 0\n"
    "v_mov_b32 v116, 0\n"
    "v_mov_b32 v117, 0\n"
    "v_mov_b32 v118, 0\n"
    "v_mov_b32 v119, 0\n"
    "v_mov_b32 v120, 0\n"
    "v_mov_b32 v121, 0\n"
    "v_mov_b32 v122, 0\n"
    "v_mov_b32 v123, 0\n"
    "v_mov_b32 v72, v96\n"
    "v_mov_b32 v73, v97\n"
    "s_waitcnt vmcnt(0)\n"
    "s_branch CHK82_%=\n"
    "RT80_%=:\n"
    "s_sub_u32 s26, s26, 1\n"
    "s_cmp_lt_u32 s26, 0\n"
    "s_cbranch_scc0 NS83_%=\n"
    "s_sleep 4\n"
    "NS83_%=:\n"
    "s_mov_b64 exec, s[18:19]\n"
    "buffer_load_dwordx4 v[24:27], v99, s[40:43], s60 offen sc1\n"
    "s_mov_b64 exec, s[20:21]\n"
    "buffer_load_dwordx4 v[28:31], v99, s[40:43], s61 offen sc1\n"
    "s_mov_b64 exec, s[22:23]\n"
    "buffer_load_dwordx4 v[32:35], v99, s[40:43], s4 offen sc1\n"
    "s_mov_b64 exec, s[24:25]\n"
    "buffer_load_dwordx4 v[36:39], v99, s[40:43], s5 offen sc1\n"
    "s_mov_b64 exec, -1\n"
    "s_add_i32 s64, s64, -1\n"
    "s_cmp_gt_i32 s64, 0\n"
    "s_cselect_b64 s[66:67], -1, 0\n"
    "MID81_%=:\n"
    "v_mov_b32 v72, v96\n"
    "v_mov_b32 v73, v97\n"
    "s_waitcnt vmcnt(0)\n"
    "CHK82_%=:\n"
    "v_max3_u32 v16, v24, v25, v26\n"
    "v_max_u32 v16, v16, v27\n"
    "v_max3_u32 v17, v28, v29, v30\n"
    "v_max_u32 v17, v17, v31\n"
    "v_smfmac_f32_16x16x64_bf16 v[72:75], v[6:9], v[24:31], v4\n"
    "v_max3_u32 v18, v32, v33, v34\n"
    "v_max_u32 v18, v18, v35\n"
    "v_max3_u32 v19, v36, v37, v38\n"
    "v_max_u32 v19, v19, v39\n"
    "v_smfmac_f32_16x16x64_bf16 v[72:75], v[6:9], v[32:39], v4\n"
    "v_cmp_eq_u32 s[18:19], -1, v16\n"
    "v_cmp_eq_u32 s[20:21], -1, v17\n"
    "v_cmp_eq_u32 s[22:23], -1, v18\n"
    "s_or_b64 vcc, s[18:19], s[20:21]\n"
    "v_cmp_eq_u32 s[24:25], -1, v19\n"
    "s_or_b64 vcc, vcc, s[22:23]\n"
    "s_or_b64 vcc, vcc, s[24:25]\n"
    "s_and_b64 vcc, vcc, s[66:67]\n"
    "s_cbranch_scc1 RT80_%=\n"
    "v_cvt_pk_bf16_f32 v16, v72, v73\n"
    "v_dot2_f32_bf16 v17, v16, v105, 0\n"
    "v_dot2_f32_bf16 v18, v16, v16, 0\n"
    "buffer_store_dword v16, v100, s[40:43], s55 offen sc1\n"
    "v_mov_b32_dpp v76, v16 row_newbcast:0 row_mask:0xf bank_mask:0x7\n"
    "v_mov_b32_dpp v77, v16 row_newbcast:1 row_mask:0xf bank_mask:0x7\n"
    "v_mov_b32_dpp v78, v16 row_newbcast:2 row_mask:0xf bank_mask:0x7\n"
    "v_mov_b32_dpp v79, v16 row_newbcast:3 row_mask:0xf bank_mask:0x7\n"
    "v_permlane32_swap_b32 v17, v18\n"
    "v_mov_b32_dpp v80, v16 row_newbcast:4 row_mask:0xf bank_mask:0x7\n"
    "v_smfmac_f32_16x16x64_bf16 v[92:95], v[76:79], v[192:199], v2\n"
    "v_add_f32 v19, v17, v18\n"
    "v_mov_b32_dpp v81, v16 row_newbcast:5 row_mask:0xf bank_mask:0x7\n"
    "v_mov_b32_dpp v82, v16 row_newbcast:6 row_mask:0xf bank_mask:0x7\n"
    "v_mov_b32_dpp v83, v16 row_newbcast:7 row_mask:0xf bank_mask:0x7\n"
    "v_add_f32_dpp v19, v19, v19 row_ror:8 row_mask:0xf bank_mask:0xf\n"
    "v_mov_b32_dpp v84, v16 row_newbcast:8 row_mask:0xf bank_mask:0x7\n"
    "v_smfmac_f32_16x16x64_bf16 v[92:95], v[80:83], v[200:207], v2\n"
    "v_add_f32_dpp v19, v19, v19 row_ror:4 row_mask:0xf bank_mask:0xf\n"
    "v_mov_b32_dpp v85, v16 row_newbcast:9 row_mask:0xf bank_mask:0x7\n"
    "v_mov_b32_dpp v86, v16 row_newbcast:10 row_mask:0xf bank_mask:0x7\n"
    "v_mov_b32_dpp v87, v16 row_newbcast:11 row_mask:0xf bank_mask:0x7\n"
    "v_add_f32_dpp v19, v19, v19 row_ror:2 row_mask:0xf bank_mask:0xf\n"
    "v_mov_b32_dpp v88, v16 row_newbcast:12 row_mask:0xf bank_mask:0x7\n"
    "v_smfmac_f32_16x16x64_bf16 v[92:95], v[84:87], v[208:215], v2\n"
    "v_add_f32_dpp v19, v19, v19 row_ror:1 row_mask:0xf bank_mask:0xf\n"
    "v_mov_b32_dpp v89, v16 row_newbcast:13 row_mask:0xf bank_mask:0x7\n"
    "v_mov_b32_dpp v90, v16 row_newbcast:14 row_mask:0xf bank_mask:0x7\n"
    "v_mov_b32_dpp v91, v16 row_newbcast:15 row_mask:0xf bank_mask:0x7\n"
    "v_add_f32_dpp v19, v19, v19 row_bcast:15 row_mask:0xa bank_mask:0xf\n"
    "s_nop 0\n"
    "v_smfmac_f32_16x16x64_bf16 v[92:95], v[88:91], v[216:223], v2\n"
    "s_mov_b64 exec, s[58:59]\n"
    "ds_write_b32 v101, v19\n"
    "s_mov_b64 exec, -1\n"
    "s_nop 4\n"
    "ds_write2_b32 v125, v92, v93 offset1:2\n"
    "s_waitcnt lgkmcnt(0)\n"
    "s_barrier\n"
    "ds_read_b128 v[64:67], v103\n"
    "ds_read_b64 v[68:69], v124\n"
    "ds_read_b64 v[18:19], v102\n"
    "ds_read_b64 v[70:71], v102 offset:512\n"
    "ds_read_b64 v[72:73], v102 offset:1024\n"
    "s_waitcnt lgkmcnt(3)\n"
    "v_pk_add_f32 v[16:17], v[64:65], v[66:67]\n"
    "v_pk_add_f32 v[16:17], v[16:17], v[68:69]\n"
    "s_waitcnt lgkmcnt(0)\n"
    "v_pk_add_f32 v[18:19], v[18:19], v[70:71]\n"
    "v_pk_add_f32 v[18:19], v[18:19], v[72:73]\n"
    "v_add_f32_dpp v16, v16, v16 row_ror:8 row_mask:0xf bank_mask:0xf\n"
    "v_add_f32_dpp v17, v17, v17 row_ror:8 row_mask:0xf bank_mask:0xf\n"
    "v_pk_mul_f32 v[16:17], v[16:17], s[32:33] op_sel_hi:[1,0]\n"
    "v_fma_f32 v17, -v16, v16, v17\n"
    "v_fma_f32 v18, -v16, v19, v18\n"
    "v_fma_f32 v17, |v17|, s33, v107\n"
    "v_rsq_f32 v17, v17\n"
    "v_add_f32_dpp v18, v18, v18 row_ror:8 row_mask:0xf bank_mask:0xf\n"
    "v_fma_f32 v18, v18, v17, v98\n"
    "v_mul_f32 v19, v18, v18\n"
    "v_fmamk_f32 v19, v19, 0xbdd2d3e8, v106\n"
    "v_mul_f32 v19, v19, v18\n"
    "v_exp_f32 v19, v19\n"
    "s_nop 0\n"
    "v_add_f32 v19, 1.0, v19\n"
    "v_rcp_f32 v19, v19\n"
    "s_nop 0\n"
    "v_mul_f32 v18, v18, v19\n"
    "s_nop 1\n"
    "v_mov_b32_dpp v19, v18 quad_perm:[1,1,3,3] row_mask:0xf bank_mask:0xf\n"
    "v_cvt_pk_bf16_f32 v19, v18, v19\n"
    "s_nop 1\n"
    "v_mov_b32_dpp v76, v19 row_newbcast:0 row_mask:0xf bank_mask:0xf\n"
    "v_mov_b32_dpp v77, v19 row_newbcast:2 row_mask:0xf bank_mask:0xf\n"
    "v_mov_b32_dpp v78, v19 row_newbcast:4 row_mask:0xf bank_mask:0xf\n"
    "v_mov_b32_dpp v79, v19 row_newbcast:6 row_mask:0xf bank_mask:0xf\n"
    "s_nop 1\n"
    "v_smfmac_f32_16x16x64_bf16 v[108:111], v[76:79], v[224:231], v2\n"
    "v_smfmac_f32_16x16x64_bf16 v[112:115], v[76:79], v[232:239], v2\n"
    "v_smfmac_f32_16x16x64_bf16 v[116:119], v[76:79], v[240:247], v2\n"
    "v_smfmac_f32_16x16x64_bf16 v[120:123], v[76:79], v[248:255], v2\n"
    "s_nop 1\n"
    "v_cvt_pk_bf16_f32 v64, v108, v109\n"
    "s_nop 0\n"
    "v_cvt_pk_bf16_f32 v65, v112, v113\n"
    "s_nop 0\n"
    "v_cvt_pk_bf16_f32 v66, v116, v117\n"
    "s_nop 0\n"
    "v_cvt_pk_bf16_f32 v67, v120, v121\n"
    "buffer_store_dwordx4 v[64:67], v104, s[40:43], s65 offen sc1\n"
    "buffer_store_dwordx4 v[64:67], v104, s[40:43], s27 offen sc1\n"
    "s_waitcnt vmcnt(3)\n"
    "Q253_%=:\n"
    "ds_read_b32 v16, v5 offset:256\n"
    "v_xor_b32 v21, 64, v12\n"
    "v_xor_b32 v22, 0x80, v12\n"
    "v_xor_b32 v23, 0xc0, v12\n"
    "ds_read_b128 v[64:67], v12 offset:0\n"
    "ds_read_b128 v[68:71], v21 offset:0\n"
    "ds_read_b128 v[72:75], v22 offset:0\n"
    "ds_read_b128 v[76:79], v23 offset:0\n"
    "ds_read_b128 v[80:83], v12 offset:512\n"
    "ds_read_b128 v[84:87], v21 offset:512\n"
    "ds_read_b128 v[88:91], v22 offset:512\n"
    "ds_read_b128 v[92:95], v23 offset:512\n"
    "ds_read_b128 v[96:99], v12 offset:4096\n"
    "ds_read_b128 v[100:103], v21 offset:4096\n"
    "ds_read_b128 v[104:107], v22 offset:4096\n"
    "ds_read_b128 v[108:111], v23 offset:4096\n"
    "ds_read_b128 v[112:115], v12 offset:4608\n"
    "ds_read_b128 v[116:119], v21 offset:4608\n"
    "ds_read_b128 v[120:123], v22 offset:4608\n"
    "ds_read_b128 v[124:127], v23 offset:4608\n"
    "v_lshlrev_b32 v21, 3, v0\n"
    "v_add_u32 v21, 139264, v21\n"
    "v_mov_b32 v19, 0x3f803f80\n"
    "s_mul_i32 s65, s51, 1408\n"
    "s_add_u32 s65, s65, 491520\n"
    "v_mov_b32 v18, 0x40000000\n"
    "s_lshl_b32 s3, s46, 7\n"
    "v_lshl_add_u32 v20, v13, 3, s3\n"
    "v_cmp_lt_u32 vcc, 47, v0\n"
    "s_nop 1\n"
    "v_cndmask_b32 v20, v18, v20, vcc\n"
    "v_mov_b32 v34, 0\n"
    "v_mov_b32 v35, 0\n"
    "v_mov_b32 v36, 0\n"
    "v_mov_b32 v37, 0\n"
    "v_mov_b32 v38, 0\n"
    "v_mov_b32 v39, 0\n"
    "s_cmp_ge_u32 s46, 4\n"
    "s_cbranch_scc0 SK84_%=\n"
    "v_mul_u32_u24 v40, 1280, v15\n"
    "v_lshl_add_u32 v40, v13, 4, v40\n"
    "v_cmp_gt_u32 vcc, 10, v13\n"
    "s_nop 1\n"
    "v_cndmask_b32 v40, v18, v40, vcc\n"
    "s_cmp_eq_u32 s46, 7\n"
    "s_cselect_b32 s3, 48, 64\n"
    "s_cselect_b32 s54, 0, 64\n"
    "v_cmp_gt_u32 vcc, s3, v0\n"
    "s_nop 1\n"
    "v_cndmask_b32 v41, v18, v40, vcc\n"
    "v_cmp_gt_u32 vcc, s54, v0\n"
    "s_nop 1\n"
    "v_cndmask_b32 v42, v18, v40, vcc\n"
    "s_mul_i32 s60, s46, 20480\n"
    "s_mul_i32 s3, s50, 160\n"
    "s_add_u32 s60, s60, s3\n"
    "s_and_b32 s3, s47, 7\n"
    "s_mul_i32 s3, s3, 71680\n"
    "s_add_u32 s60, s60, s3\n"
    "s_add_u32 s60, s60, 2539520\n"
    "s_add_u32 s61, s60, 5120\n"
    "s_add_u32 s54, s60, 10240\n"
    "s_add_u32 s55, s60, 15360\n"
    "s_lshl_b32 s3, s46, 9\n"
    "s_add_u32 s3, s3, 137216\n"
    "v_lshl_add_u32 v43, v0, 3, s3\n"
    "s_cmp_eq_u32 s46, 4\n"
    "s_cselect_b32 s3, -1, 0\n"
    "s_waitcnt lgkmcnt(15)\n"
    "v_and_b32 v45, s3, v16\n"
    "v_lshlrev_b32 v44, 16, v45\n"
    "v_and_b32 v45, 0xffff0000, v45\n"
    "v_mov_b32 v46, 152752\n"
    "s_cmp_eq_u32 s52, 2\n"
    "s_cbranch_scc1 GM85_%=\n"
    "s_cmp_eq_u32 s46, 7\n"
    "s_cbranch_scc0 SK87_%=\n"
    "s_mov_b32 s4, 0\n"
    "s_mov_b32 s5, 1\n"
    "v_mov_b32 v47, -1\n"
    "s_mov_b32 s26, 0\n"
    "s_mov_b64 s[66:67], -1\n"
    "s_mov_b64 exec, s[4:5]\n"
    "buffer_load_dword v47, v41, s[40:43], s61 offen sc1\n"
    "s_mov_b64 exec, -1\n"
    "s_branch MID89_%=\n"
    "RT88_%=:\n"
    "s_sub_u32 s26, s26, 1\n"
    "s_cmp_lt_u32 s26, 0\n"
    "s_cbranch_scc0 NS91_%=\n"
    "s_sleep 6\n"
    "NS91_%=:\n"
    "s_mov_b64 exec, s[18:19]\n"
    "buffer_load_dword v47, v41, s[40:43], s61 offen sc1\n"
    "s_mov_b64 exec, -1\n"
    "s_add_i32 s64, s64, -1\n"
    "s_cmp_gt_i32 s64, 0\n"
    "s_cselect_b64 s[66:67], -1, 0\n"
    "MID89_%=:\n"
    "s_waitcnt vmcnt(0)\n"
    "CHK90_%=:\n"
    "v_cmp_eq_u32 s[18:19], -1, v47\n"
    "s_and_b64 s[18:19], s[18:19], s[4:5]\n"
    "s_mov_b64 vcc, s[18:19]\n"
    "s_and_b64 vcc, vcc, s[66:67]\n"
    "s_cbranch_scc1 RT88_%=\n"
    "ds_write_b32 v46, v46\n"
    "s_branch GD86_%=\n"
    "SK87_%=:\n"
    "s_branch IN93_%=\n"
    "RT92_%=:\n"
    "s_sleep 1\n"
    "IN93_%=:\n"
    "s_add_i32 s64, s64, -1\n"
    "s_cmp_gt_i32 s64, 0\n"
    "s_cselect_b64 s[66:67], -1, 0\n"
    "ds_read_b32 v47, v46\n"
    "s_waitcnt lgkmcnt(0)\n"
    "v_cmp_eq_u32 vcc, 0, v47\n"
    "s_and_b64 vcc, vcc, s[66:67]\n"
    "s_cbranch_scc1 RT92_%=\n"
    "s_branch GD86_%=\n"
    "GM85_%=:\n"
    "s_cmp_ge_u32 s46, 5\n"
    "s_cbranch_scc0 SK94_%=\n"
    "s_sleep 8\n"
    "SK94_%=:\n"
    "GD86_%=:\n"
    "v_mov_b32 v24, -1\n"
    "v_mov_b32 v25, -1\n"
    "v_mov_b32 v26, -1\n"
    "v_mov_b32 v27, -1\n"
    "v_mov_b32 v28, -1\n"
    "v_mov_b32 v29, -1\n"
    "v_mov_b32 v30, -1\n"
    "v_mov_b32 v31, -1\n"
    "v_mov_b32 v32, -1\n"
    "v_mov_b32 v33, -1\n"
    "v_mov_b32 v34, -1\n"
    "v_mov_b32 v35, -1\n"
    "v_mov_b32 v36, -1\n"
    "v_mov_b32 v37, -1\n"
    "v_mov_b32 v38, -1\n"
    "v_mov_b32 v39, -1\n"
    "s_mov_b32 s26, 1\n"
    "s_mov_b64 s[66:67], -1\n"
    "s_sleep 8\n"
    "buffer_load_dwordx4 v[24:27], v40, s[40:43], s60 offen sc1\n"
    "buffer_load_dwordx4 v[28:31], v41, s[40:43], s61 offen sc1\n"
    "buffer_load_dwordx4 v[32:35], v42, s[40:43], s54 offen sc1\n"
    "buffer_load_dwordx4 v[36:39], v42, s[40:43], s55 offen sc1\n"
    "v_mov_b32 v56, v44\n"
    "v_mov_b32 v57, v45\n"
    "s_waitcnt vmcnt(0)\n"
    "s_branch CHK97_%=\n"
    "RT95_%=:\n"
    "s_sub_u32 s26, s26, 1\n"
    "s_cmp_lt_u32 s26, 0\n"
    "s_cbranch_scc0 NS98_%=\n"
    "s_sleep 4\n"
    "NS98_%=:\n"
    "s_mov_b64 exec, s[18:19]\n"
    "buffer_load_dwordx4 v[24:27], v40, s[40:43], s60 offen sc1\n"
    "s_mov_b64 exec, s[20:21]\n"
    "buffer_load_dwordx4 v[28:31], v41, s[40:43], s61 offen sc1\n"
    "s_mov_b64 exec, s[22:23]\n"
    "buffer_load_dwordx4 v[32:35], v42, s[40:43], s54 offen sc1\n"
    "s_mov_b64 exec, s[24:25]\n"
    "buffer_load_dwordx4 v[36:39], v42, s[40:43], s55 offen sc1\n"
    "s_mov_b64 exec, -1\n"
    "s_add_i32 s64, s64, -1\n"
    "s_cmp_gt_i32 s64, 0\n"
    "s_cselect_b64 s[66:67], -1, 0\n"
    "MID96_%=:\n"
    "v_mov_b32 v56, v44\n"
    "v_mov_b32 v57, v45\n"
    "s_waitcnt vmcnt(0)\n"
    "CHK97_%=:\n"
    "v_max3_u32 v48, v24, v25, v26\n"
    "v_max_u32 v48, v48, v27\n"
    "v_max3_u32 v49, v28, v29, v30\n"
    "v_max_u32 v49, v49, v31\n"
    "v_smfmac_f32_16x16x64_bf16 v[56:59], v[6:9], v[24:31], v4\n"
    "v_max3_u32 v50, v32, v33, v34\n"
    "v_max_u32 v50, v50, v35\n"
    "v_max3_u32 v51, v36, v37, v38\n"
    "v_max_u32 v51, v51, v39\n"
    "v_smfmac_f32_16x16x64_bf16 v[56:59], v[6:9], v[32:39], v4\n"
    "v_cmp_eq_u32 s[18:19], -1, v48\n"
    "v_cmp_eq_u32 s[20:21], -1, v49\n"
    "v_cmp_eq_u32 s[22:23], -1, v50\n"
    "s_or_b64 vcc, s[18:19], s[20:21]\n"
    "v_cmp_eq_u32 s[24:25], -1, v51\n"
    "s_or_b64 vcc, vcc, s[22:23]\n"
    "s_or_b64 vcc, vcc, s[24:25]\n"
    "s_and_b64 vcc, vcc, s[66:67]\n"
    "s_cbranch_scc1 RT95_%=\n"
    "ds_write_b64 v43, v[56:57]\n"
    "SK84_%=:\n"
    "s_waitcnt lgkmcnt(0)\n"
    "s_barrier\n"
    "s_cmp_lt_u32 s46, 4\n"
    "s_cbranch_scc0 SK99_%=\n"
    "ds_read_b64 v[24:25], v21 offset:0\n"
    "ds_read_b64 v[26:27], v21 offset:512\n"
    "ds_read_b64 v[28:29], v21 offset:1024\n"
    "ds_read_b64 v[30:31], v21 offset:1536\n"
    "s_waitcnt lgkmcnt(2)\n"
    "v_pk_add_f32 v[16:17], v[24:25], v[26:27]\n"
    "s_waitcnt lgkmcnt(0)\n"
    "v_pk_add_f32 v[28:29], v[28:29], v[30:31]\n"
    "v_pk_add_f32 v[16:17], v[16:17], v[28:29]\n"
    "v_cvt_pk_bf16_f32 v16, v16, v17\n"
    "v_dot2_f32_bf16 v18, v16, v19, 0\n"
    "s_nop 0\n"
    "v_mov_b32_dpp v24, v16 row_newbcast:0 row_mask:0xf bank_mask:0xf\n"
    "v_mov_b32_dpp v25, v16 row_newbcast:1 row_mask:0xf bank_mask:0xf\n"
    "v_mov_b32_dpp v26, v16 row_newbcast:2 row_mask:0xf bank_mask:0xf\n"
    "v_mov_b32_dpp v27, v16 row_newbcast:3 row_mask:0xf bank_mask:0xf\n"
    "v_add_f32_dpp v18, v18, v18 row_ror:8 row_mask:0xf bank_mask:0xf\n"
    "v_mov_b32_dpp v28, v16 row_newbcast:4 row_mask:0xf bank_mask:0xf\n"
    "v_smfmac_f32_16x16x64_bf16 v[36:39], v[24:27], v[40:47], v2\n"
    "v_add_f32_dpp v18, v18, v18 row_ror:4 row_mask:0xf bank_mask:0xf\n"
    "v_mov_b32_dpp v29, v16 row_newbcast:5 row_mask:0xf bank_mask:0xf\n"
    "v_mov_b32_dpp v30, v16 row_newbcast:6 row_mask:0xf bank_mask:0xf\n"
    "v_add_f32_dpp v18, v18, v18 row_ror:2 row_mask:0xf bank_mask:0xf\n"
    "v_mov_b32_dpp v31, v16 row_newbcast:7 row_mask:0xf bank_mask:0xf\n"
    "v_mov_b32_dpp v32, v16 row_newbcast:8 row_mask:0xf bank_mask:0xf\n"
    "v_add_f32_dpp v18, v18, v18 row_ror:1 row_mask:0xf bank_mask:0xf\n"
    "v_mov_b32_dpp v33, v16 row_newbcast:9 row_mask:0xf bank_mask:0xf\n"
    "v_smfmac_f32_16x16x64_bf16 v[36:39], v[28:31], v[48:55], v2\n"
    "v_add_f32_dpp v18, v18, v18 row_bcast:15 row_mask:0xa bank_mask:0xf\n"
    "v_smfmac_f32_16x16x64_bf16 v[36:39], v[32:35], v[56:63], v2\n"
    "s_nop 0\n"
    "v_add_f32_dpp v18, v18, v18 row_bcast:31 row_mask:0xc bank_mask:0xf\n"
    "s_nop 5\n"
    "v_pk_fma_f32 v[36:37], v[18:19], v[10:11], v[36:37] op_sel_hi:[0,1,1]\n"
    "buffer_store_dwordx2 v[36:37], v20, s[40:43], s65 offen sc1\n"
    "SK99_%=:\n"
    "s_cmp_eq_u32 s46, 4\n"
    "s_cbranch_scc0 SK100_%=\n"
    "ds_read_b64 v[48:49], v21 offset:0\n"
    "ds_read_b64 v[50:51], v21 offset:512\n"
    "ds_read_b64 v[52:53], v21 offset:1024\n"
    "ds_read_b64 v[54:55], v21 offset:1536\n"
    "s_waitcnt lgkmcnt(2)\n"
    "v_pk_add_f32 v[16:17], v[48:49], v[50:51]\n"
    "s_waitcnt lgkmcnt(0)\n"
    "v_pk_add_f32 v[52:53], v[52:53], v[54:55]\n"
    "v_pk_add_f32 v[16:17], v[16:17], v[52:53]\n"
    "v_cvt_pk_bf16_f32 v16, v16, v17\n"
    "v_dot2_f32_bf16 v58, v16, v19, 0\n"
    "v_dot2_f32_bf16 v59, v16, v16, 0\n"
    "s_nop 1\n"
    "v_add_f32_dpp v58, v58, v58 row_ror:8 row_mask:0xf bank_mask:0xf\n"
    "v_add_f32_dpp v59, v59, v59 row_ror:8 row_mask:0xf bank_mask:0xf\n"
    "s_nop 0\n"
    "v_add_f32_dpp v58, v58, v58 row_ror:4 row_mask:0xf bank_mask:0xf\n"
    "v_add_f32_dpp v59, v59, v59 row_ror:4 row_mask:0xf bank_mask:0xf\n"
    "s_nop 0\n"
    "v_add_f32_dpp v58, v58, v58 row_ror:2 row_mask:0xf bank_mask:0xf\n"
    "v_add_f32_dpp v59, v59, v59 row_ror:2 row_mask:0xf bank_mask:0xf\n"
    "s_nop 0\n"
    "v_add_f32_dpp v58, v58, v58 row_ror:1 row_mask:0xf bank_mask:0xf\n"
    "v_add_f32_dpp v59, v59, v59 row_ror:1 row_mask:0xf bank_mask:0xf\n"
    "s_nop 0\n"
    "v_add_f32_dpp v58, v58, v58 row_bcast:15 row_mask:0xa bank_mask:0xf\n"
    "v_add_f32_dpp v59, v59, v59 row_bcast:15 row_mask:0xa bank_mask:0xf\n"
    "s_nop 0\n"
    "v_add_f32_dpp v58, v58, v58 row_bcast:31 row_mask:0xc bank_mask:0xf\n"
    "v_add_f32_dpp v59, v59, v59 row_bcast:31 row_mask:0xc bank_mask:0xf\n"
    "buffer_store_dwordx2 v[58:59], v20, s[40:43], s65 offen sc1\n"
    "SK100_%=:\n"
    "s_cmp_ge_u32 s46, 5\n"
    "s_cbranch_scc0 SK101_%=\n"
    "s_waitcnt vmcnt(0)\n"
    "v_mov_b32 v24, 0\n"
    "v_mov_b32 v25, 0\n"
    "v_mov_b32 v26, 0\n"
    "v_mov_b32 v27, 0\n"
    "v_mov_b32 v28, 0\n"
    "v_mov_b32 v29, 0\n"
    "v_mov_b32 v30, 0\n"
    "v_mov_b32 v31, 0\n"
    "v_mov_b32 v32, 0\n"
    "v_mov_b32 v33, 0\n"
    "v_mov_b32 v34, 0\n"
    "v_mov_b32 v35, 0\n"
    "v_mov_b32 v36, 0\n"
    "v_mov_b32 v37, 0\n"
    "v_mov_b32 v38, 0\n"
    "v_mov_b32 v39, 0\n"
    "s_lshl_b32 s3, s46, 8\n"
    "s_add_u32 s3, s3, s51\n"
    "s_sub_u32 s3, s3, 1528\n"
    "s_cmp_lt_u32 s3, 8\n"
    "s_cbranch_scc0 SK102_%=\n"
    "ds_read_b64 v[48:49], v21 offset:0\n"
    "ds_read_b64 v[50:51], v21 offset:512\n"
    "ds_read_b64 v[52:53], v21 offset:1024\n"
    "ds_read_b64 v[54:55], v21 offset:1536\n"
    "s_waitcnt lgkmcnt(2)\n"
    "v_pk_add_f32 v[16:17], v[48:49], v[50:51]\n"
    "s_waitcnt lgkmcnt(0)\n"
    "v_pk_add_f32 v[52:53], v[52:53], v[54:55]\n"
    "v_pk_add_f32 v[16:17], v[16:17], v[52:53]\n"
    "v_cvt_pk_bf16_f32 v16, v16, v17\n"
    "v_lshlrev_b32 v17, 2, v15\n"
    "v_lshl_add_u32 v17, v13, 4, v17\n"
    "v_mov_b32 v18, 0x40000000\n"
    "v_cmp_gt_u32 vcc, 10, v13\n"
    "s_nop 1\n"
    "v_cndmask_b32 v17, v18, v17, vcc\n"
    "s_mul_i32 s3, s50, 160\n"
    "s_add_u32 s3, s3, 86784\n"
    "buffer_store_dword v16, v17, s[40:43], s3 offen sc1\n"
    "SK102_%=:\n"
    "SK101_%=:\n"
    "v_mov_b32 v40, 0\n"
    "v_mov_b32 v41, 0\n"
    "v_mov_b32 v42, 0\n"
    "v_mov_b32 v43, 0\n"
    "v_mov_b32 v44, 0\n"
    "v_mov_b32 v45, 0\n"
    "v_mov_b32 v46, 0\n"
    "v_mov_b32 v47, 0\n"
    "v_mov_b32 v48, 0\n"
    "v_mov_b32 v49, 0\n"
    "v_mov_b32 v50, 0\n"
    "v_mov_b32 v51, 0\n"
    "v_mov_b32 v52, 0\n"
    "v_mov_b32 v53, 0\n"
    "v_mov_b32 v54, 0\n"
    "v_mov_b32 v55, 0\n"
    "s_and_b32 s60, s51, 0xf8\n"
    "s_mul_i32 s60, s60, 1408\n"
    "s_lshl_b32 s3, s46, 7\n"
    "s_add_u32 s60, s60, s3\n"
    "s_add_u32 s60, s60, 491520\n"
    "s_cmp_lt_u32 s46, 4\n"
    "s_cbranch_scc0 SK103_%=\n"
    "v_lshrrev_b32 v20, 3, v0\n"
    "v_mul_u32_u24 v20, 1408, v20\n"
    "v_and_b32 v21, 7, v0\n"
    "v_lshl_add_u32 v20, v21, 4, v20\n"
    "v_and_b32 v21, 4, v0\n"
    "v_lshlrev_b32 v21, 3, v21\n"
    "v_and_b32 v22, 3, v0\n"
    "v_lshl_add_u32 v21, v22, 2, v21\n"
    "v_and_b32 v22, 2, v15\n"
    "v_lshl_add_u32 v21, v22, 3, v21\n"
    "v_and_b32 v22, 1, v15\n"
    "v_lshl_add_u32 v21, v22, 1, v21\n"
    "s_lshl_b32 s3, s46, 6\n"
    "s_add_u32 s3, s3, 152320\n"
    "v_add_u32 v21, s3, v21\n"
    "v_mov_b32 v24, 0\n"
    "v_mov_b32 v25, 0\n"
    "v_mov_b32 v26, 0\n"
    "v_mov_b32 v27, 0\n"
    "v_mov_b32 v28, 0\n"
    "v_mov_b32 v29, 0\n"
    "v_mov_b32 v30, 0\n"
    "v_mov_b32 v31, 0\n"
    "v_mov_b32 v32, 0\n"
    "v_mov_b32 v33, 0\n"
    "v_mov_b32 v34, 0\n"
    "v_mov_b32 v35, 0\n"
    "v_mov_b32 v36, 0\n"
    "v_mov_b32 v37, 0\n"
    "v_mov_b32 v38, 0\n"
    "v_mov_b32 v39, 0\n"
    "v_mov_b32 v8, -1\n"
    "v_mov_b32 v9, -1\n"
    "v_mov_b32 v10, -1\n"
    "v_mov_b32 v11, -1\n"
    "s_mov_b32 s26, 0\n"
    "s_mov_b64 s[66:67], -1\n"
    "s_sleep 9\n"
    "buffer_load_dwordx4 v[8:11], v20, s[40:43], s60 offen sc1\n"
    "s_branch MID105_%=\n"
    "RT104_%=:\n"
    "s_sub_u32 s26, s26, 1\n"
    "s_cmp_lt_u32 s26, 0\n"
    "s_cbranch_scc0 NS107_%=\n"
    "s_sleep 4\n"
    "NS107_%=:\n"
    "s_mov_b64 exec, s[18:19]\n"
    "buffer_load_dwordx4 v[8:11], v20, s[40:43], s60 offen sc1\n"
    "s_mov_b64 exec, -1\n"
    "s_add_i32 s64, s64, -1\n"
    "s_cmp_gt_i32 s64, 0\n"
    "s_cselect_b64 s[66:67], -1, 0\n"
    "MID105_%=:\n"
    "s_waitcnt vmcnt(0)\n"
    "CHK106_%=:\n"
    "v_mov_b32 v16, v8\n"
    "v_mov_b32 v17, v9\n"
    "v_mov_b32 v18, v10\n"
    "v_mov_b32 v19, v11\n"
    "v_max3_u32 v22, v16, v17, v18\n"
    "v_max_u32 v22, v22, v19\n"
    "v_cmp_eq_u32 s[18:19], -1, v22\n"
    "s_mov_b64 vcc, s[18:19]\n"
    "s_and_b64 vcc, vcc, s[66:67]\n"
    "s_cbranch_scc1 RT104_%=\n"
    "v_permlane32_swap_b32 v16, v18\n"
    "v_permlane32_swap_b32 v17, v19\n"
    "v_add_f32 v16, v16, v18\n"
    "v_add_f32 v17, v17, v19\n"
    "s_nop 1\n"
    "v_permlane16_swap_b32 v16, v17\n"
    "v_add_f32 v16, v16, v17\n"
    "s_nop 1\n"
    "v_add_f32_dpp v16, v16, v16 row_ror:8 row_mask:0xf bank_mask:0xf\n"
    "v_cvt_pk_bf16_f32 v16, v16, v16\n"
    "ds_write_b16 v21, v16\n"
    "SK103_%=:\n"
    "s_cmp_eq_u32 s46, 4\n"
    "s_cbranch_scc0 SK108_%=\n"
    "v_and_b32 v20, 7, v0\n"
    "v_mul_u32_u24 v20, 1408, v20\n"
    "s_waitcnt vmcnt(1)\n"
    "v_mov_b32 v24, 0\n"
    "v_mov_b32 v25, 0\n"
    "v_mov_b32 v26, 0\n"
    "v_mov_b32 v27, 0\n"
    "v_mov_b32 v28, 0\n"
    "v_mov_b32 v29, 0\n"
    "v_mov_b32 v30, 0\n"
    "v_mov_b32 v31, 0\n"
    "v_mov_b32 v32, 0\n"
    "v_mov_b32 v33, 0\n"
    "v_mov_b32 v34, 0\n"
    "v_mov_b32 v35, 0\n"
    "v_mov_b32 v36, 0\n"
    "v_mov_b32 v37, 0\n"
    "v_mov_b32 v38, 0\n"
    "v_mov_b32 v39, 0\n"
    "v_mov_b32 v10, -1\n"
    "v_mov_b32 v11, -1\n"
    "s_mov_b32 s26, 0\n"
    "s_mov_b64 s[66:67], -1\n"
    "s_sleep 7\n"
    "buffer_load_dwordx2 v[10:11], v20, s[40:43], s60 offen sc1\n"
    "s_branch MID110_%=\n"
    "RT109_%=:\n"
    "s_sub_u32 s26, s26, 1\n"
    "s_cmp_lt_u32 s26, 0\n"
    "s_cbranch_scc0 NS112_%=\n"
    "s_sleep 4\n"
    "NS112_%=:\n"
    "s_mov_b64 exec, s[18:19]\n"
    "buffer_load_dwordx2 v[10:11], v20, s[40:43], s60 offen sc1\n"
    "s_mov_b64 exec, -1\n"
    "s_add_i32 s64, s64, -1\n"
    "s_cmp_gt_i32 s64, 0\n"
    "s_cselect_b64 s[66:67], -1, 0\n"
    "MID110_%=:\n"
    "s_waitcnt vmcnt(0)\n"
    "CHK111_%=:\n"
    "v_max_u32 v22, v10, v11\n"
    "v_add_f32_dpp v16, v10, v10 row_ror:4 row_mask:0xf bank_mask:0xf\n"
    "v_add_f32_dpp v17, v11, v11 row_ror:4 row_mask:0xf bank_mask:0xf\n"
    "v_cmp_eq_u32 s[18:19], -1, v22\n"
    "s_mov_b64 vcc, s[18:19]\n"
    "s_and_b64 vcc, vcc, s[66:67]\n"
    "s_cbranch_scc1 RT109_%=\n"
    "v_add_f32_dpp v16, v16, v16 row_ror:2 row_mask:0xf bank_mask:0xf\n"
    "v_add_f32_dpp v17, v17, v17 row_ror:2 row_mask:0xf bank_mask:0xf\n"
    "s_nop 0\n"
    "v_add_f32_dpp v16, v16, v16 row_ror:1 row_mask:0xf bank_mask:0xf\n"
    "v_add_f32_dpp v17, v17, v17 row_ror:1 row_mask:0xf bank_mask:0xf\n"
    "v_mul_f32 v16, 0x3acccccd, v16\n"
    "v_mul_f32 v17, 0x3acccccd, v17\n"
    "v_fma_f32 v17, -v16, v16, v17\n"
    "v_max_f32 v17, 0, v17\n"
    "v_add_f32 v17, 0x3727c5ac, v17\n"
    "v_rsq_f32 v17, v17\n"
    "s_nop 0\n"
    "ds_write_b32 v5, v17 offset:0\n"
    "SK108_%=:\n"
    "s_lshl_b32 s3, s46, 2\n"
    "s_add_u32 s3, s3, 152608\n"
    "v_mov_b32 v22, s3\n"
    "v_and_b32 v23, 7, v0\n"
    "v_lshlrev_b32 v23, 2, v23\n"
    "v_add_u32 v23, 151552, v23\n"
    "s_lshl_b32 s3, s46, 9\n"
    "s_add_u32 s3, s3, 135168\n"
    "s_waitcnt lgkmcnt(0)\n"
    "s_barrier\n"
    "ds_read_b128 v[56:59], v14\n"
    "ds_read_b128 v[60:63], v14 offset:128\n"
    "ds_read_b32 v20, v5 offset:0\n"
    "s_waitcnt lgkmcnt(2)\n"
    "v_smfmac_f32_16x16x64_bf16 v[40:43], v[56:59], v[64:71], v3\n"
    "v_smfmac_f32_16x16x64_bf16 v[44:47], v[56:59], v[80:87], v3\n"
    "v_smfmac_f32_16x16x64_bf16 v[48:51], v[56:59], v[96:103], v3\n"
    "v_smfmac_f32_16x16x64_bf16 v[52:55], v[56:59], v[112:119], v3\n"
    "s_waitcnt lgkmcnt(1)\n"
    "v_smfmac_f32_16x16x64_bf16 v[40:43], v[60:63], v[72:79], v3\n"
    "v_smfmac_f32_16x16x64_bf16 v[44:47], v[60:63], v[88:95], v3\n"
    "v_smfmac_f32_16x16x64_bf16 v[48:51], v[60:63], v[104:111], v3\n"
    "v_smfmac_f32_16x16x64_bf16 v[52:55], v[60:63], v[120:127], v3\n"
    "s_nop 1\n"
    "v_add_f32 v18, v40, v41\n"
    "s_nop 0\n"
    "v_add_f32 v19, v44, v45\n"
    "s_nop 0\n"
    "v_add_f32 v21, v48, v49\n"
    "v_max3_f32 v17, v18, v19, v21\n"
    "v_add_f32 v18, v52, v53\n"
    "v_max_f32 v17, v17, v18\n"
    "v_add_f32_dpp v16, v40, v41 quad_perm:[0,1,2,3] row_mask:0x1 bank_mask:0xf\n"
    "s_nop 0\n"
    "v_max_f32_dpp v17, v17, v17 row_ror:8 row_mask:0xf bank_mask:0xf\n"
    "v_add_f32_dpp v16, v44, v45 quad_perm:[0,1,2,3] row_mask:0x2 bank_mask:0xf\n"
    "s_nop 0\n"
    "v_max_f32_dpp v17, v17, v17 row_ror:4 row_mask:0xf bank_mask:0xf\n"
    "v_add_f32_dpp v16, v48, v49 quad_perm:[0,1,2,3] row_mask:0x4 bank_mask:0xf\n"
    "s_nop 0\n"
    "v_max_f32_dpp v17, v17, v17 row_ror:2 row_mask:0xf bank_mask:0xf\n"
    "v_add_f32_dpp v16, v52, v53 quad_perm:[0,1,2,3] row_mask:0x8 bank_mask:0xf\n"
    "s_nop 0\n"
    "v_max_f32_dpp v17, v17, v17 row_ror:1 row_mask:0xf bank_mask:0xf\n"
    "s_waitcnt lgkmcnt(0)\n"
    "v_pk_mul_f32 v[16:17], v[16:17], v[20:21] op_sel_hi:[1,0]\n"
    "v_cvt_pk_bf16_f32 v18, v16, v17\n"
    "v_lshlrev_b32 v16, 16, v18\n"
    "v_and_b32 v17, 0xffff0000, v18\n"
    "v_pk_mul_f32 v[16:17], v[16:17], s[62:63] op_sel_hi:[1,0]\n"
    "v_cvt_pk_bf16_f32 v18, v16, v17\n"
    "v_lshlrev_b32 v16, 16, v18\n"
    "v_and_b32 v17, 0xffff0000, v18\n"
    "v_pk_mul_f32 v[16:17], v[16:17], s[62:63] op_sel:[0,1] op_sel_hi:[1,1]\n"
    "v_sub_f32 v18, v16, v17\n"
    "v_exp_f32 v18, v18\n"
    "s_mov_b64 exec, 1\n"
    "ds_write_b32 v22, v17\n"
    "s_mov_b64 exec, -1\n"
    "v_mov_b32_dpp v19, v18 quad_perm:[1,1,3,3] row_mask:0xf bank_mask:0xf\n"
    "v_cvt_pk_bf16_f32 v19, v18, v19\n"
    "s_nop 1\n"
    "v_mov_b32_dpp v56, v19 row_newbcast:0 row_mask:0xf bank_mask:0xf\n"
    "v_mov_b32_dpp v57, v19 row_newbcast:2 row_mask:0xf bank_mask:0xf\n"
    "v_mov_b32_dpp v58, v19 row_newbcast:4 row_mask:0xf bank_mask:0xf\n"
    "v_mov_b32_dpp v59, v19 row_newbcast:6 row_mask:0xf bank_mask:0xf\n"
    "s_nop 1\n"
    "v_smfmac_f32_16x16x64_bf16 v[24:27], v[56:59], v[128:135], v2\n"
    "v_add_f32_dpp v20, v18, v18 row_ror:8 row_mask:0xf bank_mask:0xf\n"
    "v_mov_b32_dpp v60, v19 row_newbcast:8 row_mask:0xf bank_mask:0xf\n"
    "v_smfmac_f32_16x16x64_bf16 v[28:31], v[56:59], v[136:143], v2\n"
    "v_mov_b32_dpp v61, v19 row_newbcast:10 row_mask:0xf bank_mask:0xf\n"
    "v_add_f32_dpp v20, v20, v20 row_ror:4 row_mask:0xf bank_mask:0xf\n"
    "v_smfmac_f32_16x16x64_bf16 v[32:35], v[56:59], v[144:151], v2\n"
    "v_mov_b32_dpp v62, v19 row_newbcast:12 row_mask:0xf bank_mask:0xf\n"
    "v_mov_b32_dpp v63, v19 row_newbcast:14 row_mask:0xf bank_mask:0xf\n"
    "v_smfmac_f32_16x16x64_bf16 v[36:39], v[56:59], v[152:159], v2\n"
    "v_add_f32_dpp v20, v20, v20 row_ror:2 row_mask:0xf bank_mask:0xf\n"
    "v_lshl_add_u32 v40, v0, 3, s3\n"
    "v_smfmac_f32_16x16x64_bf16 v[24:27], v[60:63], v[160:167], v2\n"
    "v_add_f32_dpp v20, v20, v20 row_ror:1 row_mask:0xf bank_mask:0xf\n"
    "v_smfmac_f32_16x16x64_bf16 v[28:31], v[60:63], v[168:175], v2\n"
    "v_add_f32_dpp v20, v20, v20 row_bcast:15 row_mask:0xa bank_mask:0xf\n"
    "v_smfmac_f32_16x16x64_bf16 v[32:35], v[60:63], v[176:183], v2\n"
    "v_add_f32_dpp v20, v20, v20 row_bcast:31 row_mask:0xc bank_mask:0xf\n"
    "v_smfmac_f32_16x16x64_bf16 v[36:39], v[60:63], v[184:191], v2\n"
    "s_mov_b64 exec, s[56:57]\n"
    "ds_write_b32 v22, v20 offset:64\n"
    "s_mov_b64 exec, -1\n"
    "v_mov_b32_dpp v18, v24 quad_perm:[0,1,2,3] row_mask:0x1 bank_mask:0xf\n"
    "v_mov_b32_dpp v19, v25 quad_perm:[0,1,2,3] row_mask:0x1 bank_mask:0xf\n"
    "v_mov_b32_dpp v18, v28 quad_perm:[0,1,2,3] row_mask:0x2 bank_mask:0xf\n"
    "v_mov_b32_dpp v19, v29 quad_perm:[0,1,2,3] row_mask:0x2 bank_mask:0xf\n"
    "v_mov_b32_dpp v18, v32 quad_perm:[0,1,2,3] row_mask:0x4 bank_mask:0xf\n"
    "v_mov_b32_dpp v19, v33 quad_perm:[0,1,2,3] row_mask:0x4 bank_mask:0xf\n"
    "v_mov_b32_dpp v18, v36 quad_perm:[0,1,2,3] row_mask:0x8 bank_mask:0xf\n"
    "v_mov_b32_dpp v19, v37 quad_perm:[0,1,2,3] row_mask:0x8 bank_mask:0xf\n"
    "ds_write_b64 v40, v[18:19]\n"
    "s_cmp_lt_u32 s46, 2\n"
    "s_cbranch_scc0 SK113_%=\n"
    "s_branch IN115_%=\n"
    "RT114_%=:\n"
    "IN115_%=:\n"
    "s_add_i32 s64, s64, -1\n"
    "s_cmp_gt_i32 s64, 0\n"
    "s_cselect_b64 s[66:67], -1, 0\n"
    "ds_read_b32 v21, v23 offset:1056\n"
    "v_and_b32 v41, 3, v0\n"
    "v_lshlrev_b32 v41, 7, v41\n"
    "v_and_b32 v42, 60, v0\n"
    "v_lshl_add_u32 v41, v42, 1, v41\n"
    "v_add_u32 v41, 135168, v41\n"
    "v_lshlrev_b32 v42, 2, v0\n"
    "s_mul_i32 s65, s51, 1408\n"
    "s_add_u32 s65, s65, 492544\n"
    "s_waitcnt lgkmcnt(0)\n"
    "v_cmp_eq_u32 vcc, -1, v21\n"
    "v_max_f32_dpp v16, v21, v21 row_ror:4 row_mask:0xf bank_mask:0xf\n"
    "s_and_b64 vcc, vcc, s[66:67]\n"
    "s_cbranch_scc1 RT114_%=\n"
    "v_max_f32_dpp v16, v16, v16 row_ror:2 row_mask:0xf bank_mask:0xf\n"
    "s_nop 1\n"
    "v_max_f32_dpp v16, v16, v16 row_ror:1 row_mask:0xf bank_mask:0xf\n"
    "v_sub_f32 v21, v21, v16\n"
    "v_exp_f32 v21, v21\n"
    "s_nop 0\n"
    "v_readlane_b32 s18, v21, 0\n"
    "v_readlane_b32 s19, v21, 1\n"
    "v_readlane_b32 s20, v21, 2\n"
    "v_readlane_b32 s21, v21, 3\n"
    "v_readlane_b32 s22, v21, 4\n"
    "v_readlane_b32 s23, v21, 5\n"
    "v_readlane_b32 s24, v21, 6\n"
    "v_readlane_b32 s25, v21, 7\n"
    "SK113_%=:\n"
    "s_waitcnt lgkmcnt(0)\n"
    "s_barrier\n"
    "s_cmp_eq_u32 s46, 0\n"
    "s_cbranch_scc0 SK116_%=\n"
    "ds_read_b64 v[24:25], v41 offset:0\n"
    "ds_read_b64 v[26:27], v41 offset:512\n"
    "ds_read_b64 v[28:29], v41 offset:1024\n"
    "ds_read_b64 v[30:31], v41 offset:1536\n"
    "ds_read_b64 v[32:33], v41 offset:2048\n"
    "ds_read_b64 v[34:35], v41 offset:2560\n"
    "ds_read_b64 v[36:37], v41 offset:3072\n"
    "ds_read_b64 v[38:39], v41 offset:3584\n"
    "s_waitcnt lgkmcnt(5)\n"
    "v_pk_mul_f32 v[16:17], v[24:25], s[18:19] op_sel_hi:[1,0]\n"
    "v_pk_fma_f32 v[16:17], v[26:27], s[18:19], v[16:17] op_sel:[0,1,0] op_sel_hi:[1,1,1]\n"
    "v_pk_fma_f32 v[16:17], v[28:29], s[20:21], v[16:17] op_sel_hi:[1,0,1]\n"
    "s_waitcnt lgkmcnt(2)\n"
    "v_pk_fma_f32 v[16:17], v[30:31], s[20:21], v[16:17] op_sel:[0,1,0] op_sel_hi:[1,1,1]\n"
    "v_pk_fma_f32 v[16:17], v[32:33], s[22:23], v[16:17] op_sel_hi:[1,0,1]\n"
    "v_pk_fma_f32 v[16:17], v[34:35], s[22:23], v[16:17] op_sel:[0,1,0] op_sel_hi:[1,1,1]\n"
    "s_waitcnt lgkmcnt(0)\n"
    "v_pk_fma_f32 v[16:17], v[36:37], s[24:25], v[16:17] op_sel_hi:[1,0,1]\n"
    "v_pk_fma_f32 v[16:17], v[38:39], s[24:25], v[16:17] op_sel:[0,1,0] op_sel_hi:[1,1,1]\n"
    "v_cvt_pk_bf16_f32 v16, v16, v17\n"
    "buffer_store_dword v16, v42, s[40:43], s65 offen sc1\n"
    "SK116_%=:\n"
    "s_cmp_eq_u32 s46, 1\n"
    "s_cbranch_scc0 SK117_%=\n"
    "ds_read_b32 v17, v23 offset:1120\n"
    "s_waitcnt lgkmcnt(0)\n"
    "v_mul_f32 v17, v17, v21\n"
    "s_nop 1\n"
    "v_add_f32_dpp v17, v17, v17 row_ror:4 row_mask:0xf bank_mask:0xf\n"
    "s_nop 1\n"
    "v_add_f32_dpp v17, v17, v17 row_ror:2 row_mask:0xf bank_mask:0xf\n"
    "s_nop 1\n"
    "v_add_f32_dpp v17, v17, v17 row_ror:1 row_mask:0xf bank_mask:0xf\n"
    "s_mov_b64 exec, 1\n"
    "buffer_store_dwordx2 v[16:17], v0, s[40:43], s65 offen offset:256 sc1\n"
    "s_mov_b64 exec, -1\n"
    "SK117_%=:\n"
    "s_cmp_eq_u32 s52, 2\n"
    "s_cbranch_scc1 M255_%=\n"
    "s_cmp_eq_u32 s52, 1\n"
    "s_cbranch_scc0 X57_%=\n"
    "s_cmp_lt_u32 s46, 4\n"
    "s_cbranch_scc0 X57_%=\n"
    "v_lshrrev_b32 v16, 2, v13\n"
    "v_lshlrev_b32 v16, 1, v16\n"
    "v_and_b32 v17, 1, v0\n"
    "v_add_u32 v16, v16, v17\n"
    "v_mul_u32_u24 v88, 1408, v16\n"
    "v_and_b32 v17, 2, v0\n"
    "v_lshl_add_u32 v16, v17, 1, v15\n"
    "v_lshl_add_u32 v88, v16, 4, v88\n"
    "v_lshrrev_b32 v17, 1, v17\n"
    "v_mul_u32_u24 v90, 0x5555, v17\n"
    "v_add_u32 v90, 0x8888, v90\n"
    "v_lshrrev_b32 v16, 3, v0\n"
    "v_mul_u32_u24 v89, 1408, v16\n"
    "s_lshr_b32 s3, s46, 1\n"
    "s_lshl_b32 s3, s3, 3\n"
    "s_lshl_b32 s60, s47, 5\n"
    "s_add_u32 s3, s3, s60\n"
    "s_cmp_ge_u32 s48, 10\n"
    "s_cselect_b32 s60, 16, 0\n"
    "s_add_u32 s3, s3, s60\n"
    "s_mul_i32 s61, s3, 1408\n"
    "s_add_u32 s61, s61, 492544\n"
    "s_and_b32 s3, s46, 1\n"
    "s_lshl_b32 s3, s3, 7\n"
    "s_add_u32 s60, s61, s3\n"
    "s_lshl_b32 s3, s46, 8\n"
    "s_add_u32 s3, s3, 144384\n"
    "v_lshl_add_u32 v91, v15, 2, s3\n"
    "v_lshl_add_u32 v91, v13, 4, v91\n"
    "v_and_b32 v17, 31, v0\n"
    "v_lshlrev_b32 v92, 3, v17\n"
    "v_add_u32 v92, 144384, v92\n"
    "v_lshrrev_b32 v16, 5, v0\n"
    "s_cmp_ge_u32 s48, 10\n"
    "s_cselect_b32 s65, 1, 0\n"
    "s_mul_i32 s3, s65, 10\n"
    "s_sub_u32 s3, s48, s3\n"
    "s_lshl_b32 s3, s3, 7\n"
    "s_lshl_b32 s65, s65, 3\n"
    "s_add_u32 s65, s65, s47\n"
    "s_mul_i32 s65, s65, 1280\n"
    "s_add_u32 s65, s65, s3\n"
    "v_mul_u32_u24 v93, 20480, v16\n"
    "v_lshl_add_u32 v93, v17, 2, v93\n"
    "s_mul_i32 s3, s46, 40960\n"
    "s_add_u32 s65, s65, s3\n"
    "s_add_u32 s65, s65, 327680\n"
    "v_mov_b32 v64, -1\n"
    "v_mov_b32 v65, -1\n"
    "v_mov_b32 v66, -1\n"
    "v_mov_b32 v67, -1\n"
    "v_mov_b32 v68, -1\n"
    "v_mov_b32 v69, -1\n"
    "s_mov_b32 s26, 0\n"
    "s_mov_b64 s[66:67], -1\n"
    "s_sleep 11\n"
    "buffer_load_dwordx4 v[64:67], v88, s[40:43], s60 offen sc1\n"
    "buffer_load_dwordx2 v[68:69], v89, s[40:43], s61 offen offset:256 sc1\n"
    "s_branch MID119_%=\n"
    "RT118_%=:\n"
    "s_sub_u32 s26, s26, 1\n"
    "s_cmp_lt_u32 s26, 0\n"
    "s_cbranch_scc0 NS121_%=\n"
    "s_sleep 4\n"
    "NS121_%=:\n"
    "s_mov_b64 exec, s[18:19]\n"
    "buffer_load_dwordx4 v[64:67], v88, s[40:43], s60 offen sc1\n"
    "s_mov_b64 exec, s[20:21]\n"
    "buffer_load_dwordx2 v[68:69], v89, s[40:43], s61 offen offset:256 sc1\n"
    "s_mov_b64 exec, -1\n"
    "s_add_i32 s64, s64, -1\n"
    "s_cmp_gt_i32 s64, 0\n"
    "s_cselect_b64 s[66:67], -1, 0\n"
    "MID119_%=:\n"
    "v_mov_b32 v72, 0\n"
    "v_mov_b32 v73, 0\n"
    "v_mov_b32 v74, 0\n"
    "v_mov_b32 v75, 0\n"
    "v_mov_b32 v76, 0\n"
    "v_mov_b32 v77, 0\n"
    "v_mov_b32 v78, 0\n"
    "v_mov_b32 v79, 0\n"
    "v_mov_b32 v80, 0\n"
    "v_mov_b32 v81, 0\n"
    "v_mov_b32 v82, 0\n"
    "v_mov_b32 v83, 0\n"
    "v_mov_b32 v84, 0\n"
    "v_mov_b32 v85, 0\n"
    "v_mov_b32 v86, 0\n"
    "v_mov_b32 v87, 0\n"
    "s_waitcnt vmcnt(0)\n"
    "CHK120_%=:\n"
    "v_max3_u32 v18, v64, v65, v66\n"
    "v_max_u32 v18, v18, v67\n"
    "v_max_u32 v19, v68, v69\n"
    "v_smfmac_f32_16x16x64_bf16 v[72:75], v[64:67], v[224:231], v90\n"
    "v_max_f32_dpp v16, v68, v68 row_ror:8 row_mask:0xf bank_mask:0xf\n"
    "v_cmp_eq_u32 s[18:19], -1, v18\n"
    "v_mov_b32 v17, v16\n"
    "v_smfmac_f32_16x16x64_bf16 v[76:79], v[64:67], v[232:239], v90\n"
    "v_cmp_eq_u32 s[20:21], -1, v19\n"
    "v_permlane16_swap_b32 v17, v16\n"
    "v_max_f32 v16, v16, v17\n"
    "v_smfmac_f32_16x16x64_bf16 v[80:83], v[64:67], v[240:247], v90\n"
    "v_mov_b32 v17, v16\n"
    "s_or_b64 vcc, s[18:19], s[20:21]\n"
    "v_smfmac_f32_16x16x64_bf16 v[84:87], v[64:67], v[248:255], v90\n"
    "s_and_b64 vcc, vcc, s[66:67]\n"
    "s_cbranch_scc1 RT118_%=\n"
    "v_permlane32_swap_b32 v17, v16\n"
    "v_max_f32 v16, v16, v17\n"
    "v_sub_f32 v16, v68, v16\n"
    "v_exp_f32 v16, v16\n"
    "v_pk_add_f32 v[72:73], v[72:73], v[74:75]\n"
    "v_mul_f32 v17, v16, v69\n"
    "v_pk_add_f32 v[76:77], v[76:77], v[78:79]\n"
    "v_mul_f32_dpp v72, v16, v72 row_newbcast:0 row_mask:0xf bank_mask:0xf\n"
    "v_add_f32_dpp v17, v17, v17 row_ror:8 row_mask:0xf bank_mask:0xf\n"
    "v_fmac_f32_dpp v72, v16, v73 row_newbcast:8 row_mask:0xf bank_mask:0xf\n"
    "v_mov_b32 v18, v17\n"
    "v_pk_add_f32 v[80:81], v[80:81], v[82:83]\n"
    "v_mul_f32_dpp v76, v16, v76 row_newbcast:0 row_mask:0xf bank_mask:0xf\n"
    "v_permlane16_swap_b32 v18, v17\n"
    "v_add_f32 v17, v17, v18\n"
    "v_pk_add_f32 v[84:85], v[84:85], v[86:87]\n"
    "v_mov_b32 v18, v17\n"
    "v_mul_f32_dpp v80, v16, v80 row_newbcast:0 row_mask:0xf bank_mask:0xf\n"
    "v_fmac_f32_dpp v76, v16, v77 row_newbcast:8 row_mask:0xf bank_mask:0xf\n"
    "v_permlane32_swap_b32 v18, v17\n"
    "v_fmac_f32_dpp v80, v16, v81 row_newbcast:8 row_mask:0xf bank_mask:0xf\n"
    "v_mul_f32_dpp v84, v16, v84 row_newbcast:0 row_mask:0xf bank_mask:0xf\n"
    "v_fmac_f32_dpp v84, v16, v85 row_newbcast:8 row_mask:0xf bank_mask:0xf\n"
    "v_add_f32 v17, v17, v18\n"
    "v_permlane32_swap_b32 v72, v80\n"
    "v_rcp_f32 v17, v17\n"
    "v_permlane32_swap_b32 v76, v84\n"
    "v_add_f32 v72, v72, v80\n"
    "v_add_f32 v76, v76, v84\n"
    "s_nop 1\n"
    "v_permlane16_swap_b32 v72, v76\n"
    "v_add_f32 v72, v72, v76\n"
    "v_mul_f32 v72, v72, v17\n"
    "ds_write_b32 v91, v72\n"
    "s_waitcnt lgkmcnt(0)\n"
    "s_barrier\n"
    "s_cmp_lt_u32 s46, 4\n"
    "s_cbranch_scc0 SK122_%=\n"
    "ds_read_b64 v[72:73], v92 offset:0\n"
    "ds_read_b64 v[74:75], v92 offset:256\n"
    "ds_read_b64 v[76:77], v92 offset:512\n"
    "ds_read_b64 v[78:79], v92 offset:768\n"
    "s_waitcnt lgkmcnt(2)\n"
    "v_pk_add_f32 v[72:73], v[72:73], v[74:75]\n"
    "s_waitcnt lgkmcnt(0)\n"
    "v_pk_add_f32 v[76:77], v[76:77], v[78:79]\n"
    "v_pk_add_f32 v[72:73], v[72:73], v[76:77]\n"
    "v_cvt_pk_bf16_f32 v72, v72, v73\n"
    "buffer_store_dword v72, v93, s[40:43], s65 offen sc1\n"
    "SK122_%=:\n"
    "s_branch X57_%=\n"
    "M255_%=:\n"
    "s_cmp_ge_u32 s46, 5\n"
    "s_cbranch_scc0 SK123_%=\n"
    "s_waitcnt lgkmcnt(0)\n"
    "s_barrier\n"
    "s_branch F56_%=\n"
    "SK123_%=:\n"
    "s_mov_b32 s12, s28\n"
    "s_mov_b32 s13, s29\n"
    "s_mov_b32 s14, 3456\n"
    "s_mov_b32 s15, 0x20000\n"
    "v_and_b32 v16, 7, v0\n"
    "v_lshl_add_u32 v16, v15, 3, v16\n"
    "v_lshlrev_b32 v16, 1, v16\n"
    "s_lshl_b32 s3, s53, 6\n"
    "v_mov_b32 v98, 0\n"
    "buffer_load_short_d16_hi v98, v16, s[12:15], s3 offen\n"
    "v_lshlrev_b32 v16, 2, v15\n"
    "v_lshl_add_u32 v16, v13, 4, v16\n"
    "s_and_b32 s3, s53, 1\n"
    "s_lshl_b32 s3, s3, 7\n"
    "v_xor_b32 v16, s3, v16\n"
    "s_lshl_b32 s3, s46, 8\n"
    "s_add_u32 s3, s3, 86784\n"
    "s_branch IN125_%=\n"
    "RT124_%=:\n"
    "s_sleep 2\n"
    "IN125_%=:\n"
    "s_add_i32 s64, s64, -1\n"
    "s_cmp_gt_i32 s64, 0\n"
    "s_cselect_b64 s[66:67], -1, 0\n"
    "buffer_load_dword v17, v16, s[40:43], s3 offen sc1\n"
    "s_waitcnt vmcnt(0)\n"
    "v_cmp_eq_u32 vcc, -1, v17\n"
    "s_and_b64 vcc, vcc, s[66:67]\n"
    "s_cbranch_scc1 RT124_%=\n"
    "v_lshlrev_b32 v96, 16, v17\n"
    "v_and_b32 v97, 0xffff0000, v17\n"
    "v_mov_b32 v8, v6\n"
    "v_mov_b32 v9, v7\n"
    "s_and_b32 s3, s53, 1\n"
    "s_lshl_b32 s3, s3, 3\n"
    "v_xor_b32 v16, s3, v13\n"
    "v_mul_u32_u24 v99, 1280, v15\n"
    "v_lshl_add_u32 v99, v16, 4, v99\n"
    "s_and_b32 s60, s47, 7\n"
    "s_mul_i32 s60, s60, 20480\n"
    "s_lshl_b32 s3, s46, 8\n"
    "s_add_u32 s60, s60, s3\n"
    "s_add_u32 s60, s60, 327680\n"
    "s_add_u32 s61, s60, 5120\n"
    "s_add_u32 s4, s60, 10240\n"
    "s_add_u32 s5, s60, 15360\n"
    "v_lshlrev_b32 v100, 2, v15\n"
    "v_lshl_add_u32 v100, v16, 4, v100\n"
    "s_add_u32 s55, s3, 157184\n"
    "s_mul_i32 s54, s53, 71680\n"
    "s_cmp_lt_u32 s53, 1\n"
    "s_cselect_b32 s54, s54, 0x40000000\n"
    "v_add_u32 v100, s54, v100\n"
    "s_lshl_b32 s54, s46, 3\n"
    "s_add_u32 s54, s54, 152768\n"
    "v_lshrrev_b32 v101, 5, v0\n"
    "v_lshl_add_u32 v101, v101, 2, s54\n"
    "v_lshrrev_b32 v17, 3, v13\n"
    "v_mul_u32_u24 v102, 1536, v17\n"
    "v_lshl_add_u32 v102, v15, 6, v102\n"
    "v_and_b32 v16, 7, v0\n"
    "v_lshl_add_u32 v102, v16, 3, v102\n"
    "v_add_u32 v102, 144384, v102\n"
    "v_lshlrev_b32 v103, 4, v17\n"
    "v_add_u32 v103, 152768, v103\n"
    "v_lshlrev_b32 v124, 3, v17\n"
    "v_add_u32 v124, 152800, v124\n"
    "s_lshl_b32 s54, s46, 9\n"
    "s_add_u32 s54, s54, 144384\n"
    "v_add_u32 v16, 1, v15\n"
    "v_and_b32 v16, 2, v16\n"
    "v_lshl_add_u32 v125, v16, 7, s54\n"
    "v_lshl_add_u32 v125, v13, 4, v125\n"
    "v_and_b32 v16, 1, v15\n"
    "v_lshl_add_u32 v125, v16, 2, v125\n"
    "v_lshlrev_b32 v104, 29, v15\n"
    "v_lshl_add_u32 v104, v13, 4, v104\n"
    "s_mul_i32 s65, s53, 1280\n"
    "s_add_u32 s65, s65, s3\n"
    "s_add_u32 s65, s65, 88064\n"
    "s_add_u32 s27, s65, 286720\n"
    "v_mov_b32 v105, 0x3f803f80\n"
    "v_mov_b32 v106, 0xc0135761\n"
    "v_mov_b32 v107, 0x2edbe6ff\n"
    "v_mov_b32 v24, -1\n"
    "v_mov_b32 v25, -1\n"
    "v_mov_b32 v26, -1\n"
    "v_mov_b32 v27, -1\n"
    "v_mov_b32 v28, -1\n"
    "v_mov_b32 v29, -1\n"
    "v_mov_b32 v30, -1\n"
    "v_mov_b32 v31, -1\n"
    "v_mov_b32 v32, -1\n"
    "v_mov_b32 v33, -1\n"
    "v_mov_b32 v34, -1\n"
    "v_mov_b32 v35, -1\n"
    "v_mov_b32 v36, -1\n"
    "v_mov_b32 v37, -1\n"
    "v_mov_b32 v38, -1\n"
    "v_mov_b32 v39, -1\n"
    "s_mov_b32 s26, 0\n"
    "s_mov_b64 s[66:67], -1\n"
    "s_sleep 30\n"
    "buffer_load_dwordx4 v[24:27], v99, s[40:43], s60 offen sc1\n"
    "buffer_load_dwordx4 v[28:31], v99, s[40:43], s61 offen sc1\n"
    "buffer_load_dwordx4 v[32:35], v99, s[40:43], s4 offen sc1\n"
    "buffer_load_dwordx4 v[36:39], v99, s[40:43], s5 offen sc1\n"
    "v_mov_b32 v76, v105\n"
    "v_mov_b32 v77, v105\n"
    "v_mov_b32 v78, v105\n"
    "v_mov_b32 v79, v105\n"
    "v_mov_b32 v80, v105\n"
    "v_mov_b32 v81, v105\n"
    "v_mov_b32 v82, v105\n"
    "v_mov_b32 v83, v105\n"
    "v_mov_b32 v84, v105\n"
    "v_mov_b32 v85, v105\n"
    "v_mov_b32 v86, v105\n"
    "v_mov_b32 v87, v105\n"
    "v_mov_b32 v88, v105\n"
    "v_mov_b32 v89, v105\n"
    "v_mov_b32 v90, v105\n"
    "v_mov_b32 v91, v105\n"
    "v_mov_b32 v92, 0\n"
    "v_mov_b32 v93, 0\n"
    "v_mov_b32 v94, 0\n"
    "v_mov_b32 v95, 0\n"
    "v_mov_b32 v74, 0\n"
    "v_mov_b32 v75, 0\n"
    "v_mov_b32 v108, 0\n"
    "v_mov_b32 v109, 0\n"
    "v_mov_b32 v110, 0\n"
    "v_mov_b32 v111, 0\n"
    "v_mov_b32 v112, 0\n"
    "v_mov_b32 v113, 0\n"
    "v_mov_b32 v114, 0\n"
    "v_mov_b32 v115, 0\n"
    "v_mov_b32 v116, 0\n"
    "v_mov_b32 v117, 0\n"
    "v_mov_b32 v118, 0\n"
    "v_mov_b32 v119, 0\n"
    "v_mov_b32 v120, 0\n"
    "v_mov_b32 v121, 0\n"
    "v_mov_b32 v122, 0\n"
    "v_mov_b32 v123, 0\n"
    "s_branch MID127_%=\n"
    "RT126_%=:\n"
    "s_sub_u32 s26, s26, 1\n"
    "s_cmp_lt_u32 s26, 0\n"
    "s_cbranch_scc0 NS129_%=\n"
    "s_sleep 5\n"
    "NS129_%=:\n"
    "s_mov_b64 exec, s[18:19]\n"
    "buffer_load_dwordx4 v[24:27], v99, s[40:43], s60 offen sc1\n"
    "s_mov_b64 exec, s[20:21]\n"
    "buffer_load_dwordx4 v[28:31], v99, s[40:43], s61 offen sc1\n"
    "s_mov_b64 exec, s[22:23]\n"
    "buffer_load_dwordx4 v[32:35], v99, s[40:43], s4 offen sc1\n"
    "s_mov_b64 exec, s[24:25]\n"
    "buffer_load_dwordx4 v[36:39], v99, s[40:43], s5 offen sc1\n"
    "s_mov_b64 exec, -1\n"
    "s_add_i32 s64, s64, -1\n"
    "s_cmp_gt_i32 s64, 0\n"
    "s_cselect_b64 s[66:67], -1, 0\n"
    "MID127_%=:\n"
    "v_mov_b32 v72, v96\n"
    "v_mov_b32 v73, v97\n"
    "s_waitcnt vmcnt(0)\n"
    "CHK128_%=:\n"
    "v_max3_u32 v16, v24, v25, v26\n"
    "v_max_u32 v16, v16, v27\n"
    "v_max3_u32 v17, v28, v29, v30\n"
    "v_max_u32 v17, v17, v31\n"
    "v_smfmac_f32_16x16x64_bf16 v[72:75], v[6:9], v[24:31], v4\n"
    "v_max3_u32 v18, v32, v33, v34\n"
    "v_max_u32 v18, v18, v35\n"
    "v_max3_u32 v19, v36, v37, v38\n"
    "v_max_u32 v19, v19, v39\n"
    "v_smfmac_f32_16x16x64_bf16 v[72:75], v[6:9], v[32:39], v4\n"
    "v_cmp_eq_u32 s[18:19], -1, v16\n"
    "v_cmp_eq_u32 s[20:21], -1, v17\n"
    "v_cmp_eq_u32 s[22:23], -1, v18\n"
    "s_or_b64 vcc, s[18:19], s[20:21]\n"
    "v_cmp_eq_u32 s[24:25], -1, v19\n"
    "s_or_b64 vcc, vcc, s[22:23]\n"
    "s_or_b64 vcc, vcc, s[24:25]\n"
    "s_and_b64 vcc, vcc, s[66:67]\n"
    "s_cbranch_scc1 RT126_%=\n"
    "v_cvt_pk_bf16_f32 v16, v72, v73\n"
    "v_dot2_f32_bf16 v17, v16, v105, 0\n"
    "v_dot2_f32_bf16 v18, v16, v16, 0\n"
    "buffer_store_dword v16, v100, s[40:43], s55 offen sc1\n"
    "v_mov_b32_dpp v76, v16 row_newbcast:0 row_mask:0xf bank_mask:0x7\n"
    "v_mov_b32_dpp v77, v16 row_newbcast:1 row_mask:0xf bank_mask:0x7\n"
    "v_mov_b32_dpp v78, v16 row_newbcast:2 row_mask:0xf bank_mask:0x7\n"
    "v_mov_b32_dpp v79, v16 row_newbcast:3 row_mask:0xf bank_mask:0x7\n"
    "v_permlane32_swap_b32 v17, v18\n"
    "v_mov_b32_dpp v80, v16 row_newbcast:4 row_mask:0xf bank_mask:0x7\n"
    "v_smfmac_f32_16x16x64_bf16 v[92:95], v[76:79], v[192:199], v2\n"
    "v_add_f32 v19, v17, v18\n"
    "v_mov_b32_dpp v81, v16 row_newbcast:5 row_mask:0xf bank_mask:0x7\n"
    "v_mov_b32_dpp v82, v16 row_newbcast:6 row_mask:0xf bank_mask:0x7\n"
    "v_mov_b32_dpp v83, v16 row_newbcast:7 row_mask:0xf bank_mask:0x7\n"
    "v_add_f32_dpp v19, v19, v19 row_ror:8 row_mask:0xf bank_mask:0xf\n"
    "v_mov_b32_dpp v84, v16 row_newbcast:8 row_mask:0xf bank_mask:0x7\n"
    "v_smfmac_f32_16x16x64_bf16 v[92:95], v[80:83], v[200:207], v2\n"
    "v_add_f32_dpp v19, v19, v19 row_ror:4 row_mask:0xf bank_mask:0xf\n"
    "v_mov_b32_dpp v85, v16 row_newbcast:9 row_mask:0xf bank_mask:0x7\n"
    "v_mov_b32_dpp v86, v16 row_newbcast:10 row_mask:0xf bank_mask:0x7\n"
    "v_mov_b32_dpp v87, v16 row_newbcast:11 row_mask:0xf bank_mask:0x7\n"
    "v_add_f32_dpp v19, v19, v19 row_ror:2 row_mask:0xf bank_mask:0xf\n"
    "v_mov_b32_dpp v88, v16 row_newbcast:12 row_mask:0xf bank_mask:0x7\n"
    "v_smfmac_f32_16x16x64_bf16 v[92:95], v[84:87], v[208:215], v2\n"
    "v_add_f32_dpp v19, v19, v19 row_ror:1 row_mask:0xf bank_mask:0xf\n"
    "v_mov_b32_dpp v89, v16 row_newbcast:13 row_mask:0xf bank_mask:0x7\n"
    "v_mov_b32_dpp v90, v16 row_newbcast:14 row_mask:0xf bank_mask:0x7\n"
    "v_mov_b32_dpp v91, v16 row_newbcast:15 row_mask:0xf bank_mask:0x7\n"
    "v_add_f32_dpp v19, v19, v19 row_bcast:15 row_mask:0xa bank_mask:0xf\n"
    "s_nop 0\n"
    "v_smfmac_f32_16x16x64_bf16 v[92:95], v[88:91], v[216:223], v2\n"
    "s_mov_b64 exec, s[58:59]\n"
    "ds_write_b32 v101, v19\n"
    "s_mov_b64 exec, -1\n"
    "s_nop 4\n"
    "ds_write2_b32 v125, v92, v93 offset1:2\n"
    "s_waitcnt lgkmcnt(0)\n"
    "s_barrier\n"
    "ds_read_b128 v[64:67], v103\n"
    "ds_read_b64 v[68:69], v124\n"
    "ds_read_b64 v[18:19], v102\n"
    "ds_read_b64 v[70:71], v102 offset:512\n"
    "ds_read_b64 v[72:73], v102 offset:1024\n"
    "s_waitcnt lgkmcnt(3)\n"
    "v_pk_add_f32 v[16:17], v[64:65], v[66:67]\n"
    "v_pk_add_f32 v[16:17], v[16:17], v[68:69]\n"
    "s_waitcnt lgkmcnt(0)\n"
    "v_pk_add_f32 v[18:19], v[18:19], v[70:71]\n"
    "v_pk_add_f32 v[18:19], v[18:19], v[72:73]\n"
    "v_add_f32_dpp v16, v16, v16 row_ror:8 row_mask:0xf bank_mask:0xf\n"
    "v_add_f32_dpp v17, v17, v17 row_ror:8 row_mask:0xf bank_mask:0xf\n"
    "v_pk_mul_f32 v[16:17], v[16:17], s[32:33] op_sel_hi:[1,0]\n"
    "v_fma_f32 v17, -v16, v16, v17\n"
    "v_fma_f32 v18, -v16, v19, v18\n"
    "v_fma_f32 v17, |v17|, s33, v107\n"
    "v_rsq_f32 v17, v17\n"
    "v_add_f32_dpp v18, v18, v18 row_ror:8 row_mask:0xf bank_mask:0xf\n"
    "v_fma_f32 v18, v18, v17, v98\n"
    "v_mul_f32 v19, v18, v18\n"
    "v_fmamk_f32 v19, v19, 0xbdd2d3e8, v106\n"
    "v_mul_f32 v19, v19, v18\n"
    "v_exp_f32 v19, v19\n"
    "s_nop 0\n"
    "v_add_f32 v19, 1.0, v19\n"
    "v_rcp_f32 v19, v19\n"
    "s_nop 0\n"
    "v_mul_f32 v18, v18, v19\n"
    "s_nop 1\n"
    "v_mov_b32_dpp v19, v18 quad_perm:[1,1,3,3] row_mask:0xf bank_mask:0xf\n"
    "v_cvt_pk_bf16_f32 v19, v18, v19\n"
    "s_nop 1\n"
    "v_mov_b32_dpp v76, v19 row_newbcast:0 row_mask:0xf bank_mask:0xf\n"
    "v_mov_b32_dpp v77, v19 row_newbcast:2 row_mask:0xf bank_mask:0xf\n"
    "v_mov_b32_dpp v78, v19 row_newbcast:4 row_mask:0xf bank_mask:0xf\n"
    "v_mov_b32_dpp v79, v19 row_newbcast:6 row_mask:0xf bank_mask:0xf\n"
    "s_nop 1\n"
    "v_smfmac_f32_16x16x64_bf16 v[108:111], v[76:79], v[224:231], v2\n"
    "v_smfmac_f32_16x16x64_bf16 v[112:115], v[76:79], v[232:239], v2\n"
    "v_smfmac_f32_16x16x64_bf16 v[116:119], v[76:79], v[240:247], v2\n"
    "v_smfmac_f32_16x16x64_bf16 v[120:123], v[76:79], v[248:255], v2\n"
    "s_nop 1\n"
    "v_cvt_pk_bf16_f32 v64, v108, v109\n"
    "s_nop 0\n"
    "v_cvt_pk_bf16_f32 v65, v112, v113\n"
    "s_nop 0\n"
    "v_cvt_pk_bf16_f32 v66, v116, v117\n"
    "s_nop 0\n"
    "v_cvt_pk_bf16_f32 v67, v120, v121\n"
    "buffer_store_dwordx4 v[64:67], v104, s[40:43], s65 offen sc1\n"
    "s_branch X57_%=\n"
    "F56_%=:\n"
    "s_cmp_lt_u32 s53, 10\n"
    "s_cbranch_scc0 X57_%=\n"
    "s_sub_u32 s3, s46, 5\n"
    "s_cmp_lt_u32 s3, 2\n"
    "s_cbranch_scc0 SK130_%=\n"
    "s_mov_b32 s12, s34\n"
    "s_mov_b32 s13, s35\n"
    "s_mov_b32 s14, 1280\n"
    "s_mov_b32 s15, 0x20000\n"
    "v_lshl_add_u32 v16, s3, 2, v13\n"
    "v_cmp_gt_u32 vcc, 4, v13\n"
    "s_nop 1\n"
    "v_cndmask_b32 v16, 15, v16, vcc\n"
    "v_lshl_add_u32 v16, v15, 4, v16\n"
    "v_lshlrev_b32 v16, 2, v16\n"
    "v_add_u32 v16, 152064, v16\n"
    "ds_read_b32 v21, v16\n"
    "v_mov_b32 v18, 0x40000000\n"
    "v_lshlrev_b32 v19, 2, v15\n"
    "v_lshl_add_u32 v19, v13, 4, v19\n"
    "v_cndmask_b32 v19, v18, v19, vcc\n"
    "v_and_b32 v16, 12, v0\n"
    "v_add_u32 v17, v16, v15\n"
    "v_mul_u32_u24 v16, 1280, v17\n"
    "v_and_b32 v20, 3, v0\n"
    "v_lshl_add_u32 v16, v20, 4, v16\n"
    "v_cmp_gt_u32 vcc, 7, v17\n"
    "s_nop 1\n"
    "v_cndmask_b32 v17, v18, v16, vcc\n"
    "s_lshl_b32 s65, s53, 7\n"
    "s_lshl_b32 s3, s3, 6\n"
    "s_add_u32 s65, s65, s3\n"
    "s_add_u32 s60, s65, 88064\n"
    "s_add_u32 s61, s60, 20480\n"
    "s_add_u32 s54, s60, 40960\n"
    "s_add_u32 s55, s60, 61440\n"
    "v_mov_b32 v58, 0\n"
    "v_mov_b32 v59, 0\n"
    "s_waitcnt lgkmcnt(0)\n"
    "v_lshlrev_b32 v20, 16, v21\n"
    "v_and_b32 v21, 0xffff0000, v21\n"
    "v_mov_b32 v24, -1\n"
    "v_mov_b32 v25, -1\n"
    "v_mov_b32 v26, -1\n"
    "v_mov_b32 v27, -1\n"
    "v_mov_b32 v28, -1\n"
    "v_mov_b32 v29, -1\n"
    "v_mov_b32 v30, -1\n"
    "v_mov_b32 v31, -1\n"
    "v_mov_b32 v32, -1\n"
    "v_mov_b32 v33, -1\n"
    "v_mov_b32 v34, -1\n"
    "v_mov_b32 v35, -1\n"
    "v_mov_b32 v36, -1\n"
    "v_mov_b32 v37, -1\n"
    "v_mov_b32 v38, -1\n"
    "v_mov_b32 v39, -1\n"
    "s_mov_b32 s26, 0\n"
    "s_mov_b64 s[66:67], -1\n"
    "s_sleep 9\n"
    "buffer_load_dwordx4 v[24:27], v16, s[40:43], s60 offen sc1\n"
    "buffer_load_dwordx4 v[28:31], v16, s[40:43], s61 offen sc1\n"
    "buffer_load_dwordx4 v[32:35], v16, s[40:43], s54 offen sc1\n"
    "buffer_load_dwordx4 v[36:39], v17, s[40:43], s55 offen sc1\n"
    "s_branch MID132_%=\n"
    "RT131_%=:\n"
    "s_sub_u32 s26, s26, 1\n"
    "s_cmp_lt_u32 s26, 0\n"
    "s_cbranch_scc0 NS134_%=\n"
    "s_sleep 3\n"
    "NS134_%=:\n"
    "s_mov_b64 exec, s[18:19]\n"
    "buffer_load_dwordx4 v[24:27], v16, s[40:43], s60 offen sc1\n"
    "s_mov_b64 exec, s[20:21]\n"
    "buffer_load_dwordx4 v[28:31], v16, s[40:43], s61 offen sc1\n"
    "s_mov_b64 exec, s[22:23]\n"
    "buffer_load_dwordx4 v[32:35], v16, s[40:43], s54 offen sc1\n"
    "s_mov_b64 exec, s[24:25]\n"
    "buffer_load_dwordx4 v[36:39], v17, s[40:43], s55 offen sc1\n"
    "s_mov_b64 exec, -1\n"
    "s_add_i32 s64, s64, -1\n"
    "s_cmp_gt_i32 s64, 0\n"
    "s_cselect_b64 s[66:67], -1, 0\n"
    "MID132_%=:\n"
    "v_mov_b32 v56, v20\n"
    "v_mov_b32 v57, v21\n"
    "s_waitcnt vmcnt(0)\n"
    "CHK133_%=:\n"
    "v_max3_u32 v48, v24, v25, v26\n"
    "v_max_u32 v48, v48, v27\n"
    "v_max3_u32 v49, v28, v29, v30\n"
    "v_max_u32 v49, v49, v31\n"
    "v_smfmac_f32_16x16x64_bf16 v[56:59], v[6:9], v[24:31], v4\n"
    "v_max3_u32 v50, v32, v33, v34\n"
    "v_max_u32 v50, v50, v35\n"
    "v_max3_u32 v51, v36, v37, v38\n"
    "v_max_u32 v51, v51, v39\n"
    "v_smfmac_f32_16x16x64_bf16 v[56:59], v[6:9], v[32:39], v4\n"
    "v_cmp_eq_u32 s[18:19], -1, v48\n"
    "v_cmp_eq_u32 s[20:21], -1, v49\n"
    "v_cmp_eq_u32 s[22:23], -1, v50\n"
    "s_or_b64 vcc, s[18:19], s[20:21]\n"
    "v_cmp_eq_u32 s[24:25], -1, v51\n"
    "s_or_b64 vcc, vcc, s[22:23]\n"
    "s_or_b64 vcc, vcc, s[24:25]\n"
    "s_and_b64 vcc, vcc, s[66:67]\n"
    "s_cbranch_scc1 RT131_%=\n"
    "v_add_f32_dpp v56, v56, v56 row_ror:8 row_mask:0xf bank_mask:0xf\n"
    "v_add_f32_dpp v57, v57, v57 row_ror:8 row_mask:0xf bank_mask:0xf\n"
    "s_nop 0\n"
    "v_add_f32_dpp v56, v56, v56 row_ror:4 row_mask:0xf bank_mask:0xf\n"
    "v_add_f32_dpp v57, v57, v57 row_ror:4 row_mask:0xf bank_mask:0xf\n"
    "v_cvt_pk_bf16_f32 v18, v56, v57\n"
    "buffer_store_dword v18, v19, s[12:15], s65 offen\n"
    "s_cmp_eq_u32 s65, 0\n"
    "s_cbranch_scc0 SK135_%=\n"
    "v_mov_b32 v16, s44\n"
    "v_mov_b32 v17, s45\n"
    "s_mov_b64 exec, 1\n"
    "buffer_store_dwordx2 v[16:17], v0, s[40:43], 0 offen sc1\n"
    "s_mov_b64 exec, -1\n"
    "SK135_%=:\n"
    "SK130_%=:\n"
    "X57_%=:\n"
    "LEND_%=:\n"
    : "+{v0}"(tid)
    : "{s[0:1]}"(__builtin_amdgcn_kernarg_segment_ptr()), "{s2}"(blockIdx.x)
    : "v1", "v2", "v3", "v4", "v5", "v6", "v7", "v8", "v9", "v10", "v11", "v12", "v13", "v14", "v15", "v16", "v17", "v18", "v19", "v20", "v21", "v22", "v23", "v24", "v25", "v26", "v27", "v28", "v29", "v30", "v31", "v32", "v33", "v34", "v35", "v36", "v37", "v38", "v39", "v40", "v41", "v42", "v43", "v44", "v45", "v46", "v47", "v48", "v49", "v50", "v51", "v52", "v53", "v54", "v55", "v56", "v57", "v58", "v59", "v60", "v61", "v62", "v63", "v64", "v65", "v66", "v67", "v68", "v69", "v70", "v71", "v72", "v73", "v74", "v75", "v76", "v77", "v78", "v79", "v80", "v81", "v82", "v83", "v84", "v85", "v86", "v87", "v88", "v89", "v90", "v91", "v92", "v93", "v94", "v95", "v96", "v97", "v98", "v99", "v100", "v101", "v102", "v103", "v104", "v105", "v106", "v107", "v108", "v109", "v110", "v111", "v112", "v113", "v114", "v115", "v116", "v117", "v118", "v119", "v120", "v121", "v122", "v123", "v124", "v125", "v126", "v127", "v128", "v129", "v130", "v131", "v132", "v133", "v134", "v135", "v136", "v137", "v138", "v139", "v140", "v141", "v142", "v143", "v144", "v145", "v146", "v147", "v148", "v149", "v150", "v151", "v152", "v153", "v154", "v155", "v156", "v157", "v158", "v159", "v160", "v161", "v162", "v163", "v164", "v165", "v166", "v167", "v168", "v169", "v170", "v171", "v172", "v173", "v174", "v175", "v176", "v177", "v178", "v179", "v180", "v181", "v182", "v183", "v184", "v185", "v186", "v187", "v188", "v189", "v190", "v191", "v192", "v193", "v194", "v195", "v196", "v197", "v198", "v199", "v200", "v201", "v202", "v203", "v204", "v205", "v206", "v207", "v208", "v209", "v210", "v211", "v212", "v213", "v214", "v215", "v216", "v217", "v218", "v219", "v220", "v221", "v222", "v223", "v224", "v225", "v226", "v227", "v228", "v229", "v230", "v231", "v232", "v233", "v234", "v235", "v236", "v237", "v238", "v239", "v240", "v241", "v242", "v243", "v244", "v245", "v246", "v247", "v248", "v249", "v250", "v251", "v252", "v253", "v254", "v255", "s3", "s4", "s5", "s6", "s7", "s8", "s9", "s10", "s11", "s12", "s13", "s14", "s15", "s16", "s17", "s18", "s19", "s20", "s21", "s22", "s23", "s24", "s25", "s26", "s27", "s28", "s29", "s30", "s31", "s32", "s33", "s34", "s35", "s36", "s37", "s38", "s39", "s40", "s41", "s42", "s43", "s44", "s45", "s46", "s47", "s48", "s49", "s50", "s51", "s52", "s53", "s54", "s55", "s56", "s57", "s58", "s59", "s60", "s61", "s62", "s63", "s64", "s65", "s66", "s67", "s68", "s69", "s70", "s71", "s72", "s73", "s74", "s75", "s76", "s77", "s78", "s79", "s80", "s81", "s82", "s83", "s84", "s85", "s86", "s87", "s88", "s89", "s90", "s91", "s92", "s93", "s94", "s95", "s96", "s97", "s98", "s99", "vcc", "m0", "scc", "memory");
#endif
}

extern "C" void kernel_launch(void* const* d_in, const int* in_sizes, int n_in, void* d_out, int out_size, void* d_ws, size_t ws_size, hipStream_t stream) {
  Args A;
  A.x = d_in[0]; A.K = d_in[1]; A.V = d_in[2]; A.wq = d_in[3]; A.wo = d_in[4];
  A.w1 = d_in[6]; A.b1 = d_in[7]; A.w2 = d_in[8]; A.b2 = d_in[9];
  A.out = d_out; A.ws = d_ws; A.ws2 = d_ws;
  fused_decode<<<256, 512, 0, stream>>>(A);
}
```
